```python
import torch

def _softmax(x, axis=-1):
    return torch.softmax(x, dim=axis)

def _gelu(x):
    return torch.nn.functional.gelu(x, approximate='tanh')

def _silu(x):
    return torch.nn.functional.silu(x)

def _sigmoid(x):
    return torch.sigmoid(x)

def _layernorm(x, axis=-1, eps=1e-05):
    m = x.mean(dim=axis, keepdim=True)
    v = x.var(dim=axis, keepdim=True, unbiased=False)
    return (x - m) / torch.sqrt(v + eps)

def _scatter_add(tgt, idx, src, axis=0):
    return tgt.clone().index_add_(axis, idx, src)

def _segment_sum(src, seg, n):
    out = torch.zeros((n,) + src.shape[1:], dtype=src.dtype)
    return out.index_add_(0, seg, src)

def _pad(x, pw):
    flat = tuple((v for pair in reversed(pw) for v in pair))
    return torch.nn.functional.pad(x, flat)
kernel_name = 'adaLN_cond__memory_aug_recurrent_mem_tr__geglu_ffn__prompt'

def setup_inputs():

    _w = lambda a: (lambda t: t.to(torch.bfloat16) if t.is_floating_point() else t)(torch.as_tensor(a))

    def _A_setup():
        B, S, Dm = (1, 2048, 576)
        H, Dh = (32, 128)
        M, seg = (8, 32)
        seg = min(seg, S)
        S = S // seg * seg
        return {'x': _w((lambda *s: torch.randn(*s, dtype=torch.bfloat16) * 0.05)(B, S, Dm)), 'mem0': _w((lambda *s: torch.randn(*s, dtype=torch.bfloat16) * 0.4)(B, M, Dm)), 'Wq': _w((lambda *s: torch.randn(*s, dtype=torch.bfloat16) * 3.2)(Dm, H, Dh) * Dm ** (-0.5)), 'Wk': _w((lambda *s: torch.randn(*s, dtype=torch.bfloat16) * 3.2)(Dm, H, Dh) * Dm ** (-0.5)), 'Wv': _w((lambda *s: torch.randn(*s, dtype=torch.bfloat16) * 0.4)(Dm, H, Dh) * Dm ** (-0.5)), 'Wo': _w((lambda *s: torch.randn(*s, dtype=torch.bfloat16) * 0.4)(H, Dh, Dm) * (H * Dh) ** (-0.5)), 'seg_len': seg}
    _A_in = _A_setup()

    def _B_setup():
        B, S, D, F = (1, 2048, 576, 4608)
        return {'x': _w((lambda *s: torch.randn(*s, dtype=torch.bfloat16) * 0.05)(B, S, D)), 'w_gate': _w((lambda *s: torch.randn(*s, dtype=torch.bfloat16) * 0.05)(D, F)), 'w_up': _w((lambda *s: torch.randn(*s, dtype=torch.bfloat16) * 0.05)(D, F)), 'w_down': _w((lambda *s: torch.randn(*s, dtype=torch.bfloat16) * 0.05)(F, D))}
    _B_in = _B_setup()
    return {'A_x': _A_in['x'], 'A_mem0': _A_in['mem0'], 'A_Wq': _A_in['Wq'], 'A_Wk': _A_in['Wk'], 'A_Wv': _A_in['Wv'], 'A_Wo': _A_in['Wo'], 'A_seg_len': _A_in['seg_len'], 'B_x': _B_in['x'], 'B_w_gate': _B_in['w_gate'], 'B_w_up': _B_in['w_up'], 'B_w_down': _B_in['w_down'], 'c': _w((lambda *s: torch.randn(*s, dtype=torch.bfloat16) * 0.1)(1, 576) * 8.0), 'w_mod': _w((lambda *s: torch.randn(*s, dtype=torch.bfloat16) * 0.1)(576, 6 * 576) * 576 ** (-0.5) * 8.0)}

def reference(A_x, A_mem0, A_Wq, A_Wk, A_Wv, A_Wo, A_seg_len, B_x, B_w_gate, B_w_up, B_w_down, c, w_mod):

    def _A_ref(x, mem0, Wq, Wk, Wv, Wo, seg_len):
        B, S, Dm = x.shape
        H, Dh = (Wq.shape[1], Wq.shape[2])
        M = mem0.shape[1]
        scale = Dh ** (-0.5)
        n_seg = S // seg_len
        L = M + seg_len
        idx = torch.arange(L)
        mask = (idx[:, None] < M) | (idx[:, None] >= idx[None, :])
        mem = mem0
        outs = []
        for s in range(n_seg):
            xs = x[:, s * seg_len:(s + 1) * seg_len, :].clone()
            h = torch.concatenate([mem, xs], axis=1)
            q = torch.einsum('btd,dhe->bhte', h, Wq)
            k = torch.einsum('btd,dhe->bhte', h, Wk)
            v = torch.einsum('btd,dhe->bhte', h, Wv)
            scores = torch.einsum('bhqe,bhke->bhqk', q, k) * scale
            scores = torch.where(mask[None, None], scores, -1000000000.0)
            p = _softmax(scores, axis=-1)
            o = torch.einsum('bhqk,bhke->bhqe', p, v)
            o = torch.einsum('bhte,hed->btd', o, Wo)
            mem = o[:, :M, :].clone()
            outs.append(o[:, M:, :])
        return torch.concatenate(outs, axis=1)

    def _B_ref(x, w_gate, w_up, w_down):
        return _gelu(x @ w_gate) * (x @ w_up) @ w_down
    x = A_x
    mod = (_silu(c) @ w_mod).reshape(c.shape[0], 6, 1, -1)
    sa, ca, ga, sb, cb, gb = (mod[:, 0].clone(), mod[:, 1].clone(), mod[:, 2].clone(), mod[:, 3].clone(), mod[:, 4].clone(), mod[:, 5].clone())
    h = _layernorm(x, axis=-1) * (1 + ca) + sa
    x = x + ga * _A_ref(h, A_mem0, A_Wq, A_Wk, A_Wv, A_Wo, A_seg_len)
    h = _layernorm(x, axis=-1) * (1 + cb) + sb
    x = x + gb * _B_ref(h, B_w_gate, B_w_up, B_w_down)
    return x
```

```cpp
#include <hip/hip_runtime.h>
#include <hip/hip_bf16.h>
typedef unsigned short u16;
__device__ __forceinline__ float bf2f(u16 v) { return __uint_as_float(((unsigned)v) << 16); }
__device__ __forceinline__ u16 f2bf(float f) { unsigned u = __float_as_uint(f); u += 0x7FFFu + ((u >> 16) & 1u); return (u16)(u >> 16); }
__device__ __forceinline__ float rbf(float f) { return bf2f(f2bf(f)); }

constexpr int S = 2048, D = 576, H = 32, DH = 128, HD = 4096, M = 8, SEG = 32, NSEG = 64, F = 4608, L = 40;

__global__ void k_mod(const u16* c, const u16* w, float* mod) {
  int j = blockIdx.x * blockDim.x + threadIdx.x; if (j >= 6 * D) return;
  float a = 0; for (int k = 0; k < D; ++k) { float cv = bf2f(c[k]); float s = rbf(cv / (1.f + __expf(-cv))); a += s * bf2f(w[(size_t)k * 6 * D + j]); }
  mod[j] = rbf(a);
}
__global__ void k_ln(const u16* x, const float* xf, const float* shift, const float* scale, u16* h) {
  int r = blockIdx.x; __shared__ float red[256]; int t = threadIdx.x;
  float v[3]; float s = 0;
  for (int i = 0; i < 3; ++i) { int c = t + i * 256; v[i] = c < D ? (xf ? xf[(size_t)r * D + c] : bf2f(x[(size_t)r * D + c])) : 0.f; s += v[i]; }
  red[t] = s; __syncthreads(); for (int o = 128; o > 0; o >>= 1) { if (t < o) red[t] += red[t + o]; __syncthreads(); }
  float mean = red[0] / D; __syncthreads();
  s = 0; for (int i = 0; i < 3; ++i) { int c = t + i * 256; if (c < D) { float d = v[i] - mean; s += d * d; } }
  red[t] = s; __syncthreads(); for (int o = 128; o > 0; o >>= 1) { if (t < o) red[t] += red[t + o]; __syncthreads(); }
  float var = red[0] / D; float rs = rsqrtf(var + 1e-5f);
  for (int i = 0; i < 3; ++i) { int c = t + i * 256; if (c < D) h[(size_t)r * D + c] = f2bf((v[i] - mean) * rs * (1.f + scale[c]) + shift[c]); }
}
__global__ void k_gemm(const u16* A, const u16* B, u16* C, int Mr, int N, int K, int lda, int ldb, int ldc) {
  __shared__ float sa[16][17], sb[16][17];
  int tx = threadIdx.x, ty = threadIdx.y; int row = blockIdx.y * 16 + ty, col = blockIdx.x * 16 + tx; float a = 0;
  for (int k0 = 0; k0 < K; k0 += 16) {
    sa[ty][tx] = row < Mr ? bf2f(A[(size_t)row * lda + k0 + tx]) : 0.f; sb[ty][tx] = bf2f(B[(size_t)(k0 + ty) * ldb + col]);
    __syncthreads(); for (int k = 0; k < 16; ++k) a += sa[ty][k] * sb[k][tx]; __syncthreads();
  }
  if (row < Mr) C[(size_t)row * ldc + col] = f2bf(a);
}
__global__ void k_attn(const u16* qm, const u16* km, const u16* vm, const u16* qx, const u16* kx, const u16* vx, u16* om, u16* ox) {
  int h = blockIdx.x; int r = blockIdx.y; int t = threadIdx.x;
  __shared__ float sc[L]; __shared__ float q[DH];
  const u16* qr = r < M ? qm + (size_t)r * HD : qx + (size_t)(r - M) * HD;
  q[t] = bf2f(qr[h * DH + t]); __syncthreads();
  if (t < L) { const u16* kr = t < M ? km + (size_t)t * HD : kx + (size_t)(t - M) * HD; float a = 0; for (int e = 0; e < DH; ++e) a += q[e] * bf2f(kr[h * DH + e]);
    a = rbf(rbf(a) * 0.08838834764831845f); bool ok = (r < M) || (r >= t); sc[t] = ok ? a : -1e9f; }
  __syncthreads();
  float mx = -1e30f; for (int j = 0; j < L; ++j) mx = fmaxf(mx, sc[j]); float z = 0; for (int j = 0; j < L; ++j) z += __expf(sc[j] - mx);
  float a = 0; for (int j = 0; j < L; ++j) { const u16* vr = j < M ? vm + (size_t)j * HD : vx + (size_t)(j - M) * HD; a += rbf(__expf(sc[j] - mx) / z) * bf2f(vr[h * DH + t]); }
  u16* o = r < M ? om + (size_t)r * HD : ox + (size_t)(r - M) * HD; o[h * DH + t] = f2bf(a);
}
__global__ void k_res(const u16* x, const u16* a, const float* g, float* x1) { size_t i = (size_t)blockIdx.x * 256 + threadIdx.x; if (i >= (size_t)S * D) return; int c = i % D; x1[i] = rbf(bf2f(x[i]) + rbf(g[c] * bf2f(a[i]))); }
__global__ void k_act(const u16* g, const u16* u, u16* a) { size_t i = (size_t)blockIdx.x * 256 + threadIdx.x; if (i >= (size_t)S * F) return; float x = bf2f(g[i]); float t = tanhf(0.7978845608028654f * (x + 0.044715f * x * x * x)); a[i] = f2bf(rbf(0.5f * x * (1.f + t)) * bf2f(u[i])); }
__global__ void k_res2(const float* x1, const u16* a, const float* g, u16* out) { size_t i = (size_t)blockIdx.x * 256 + threadIdx.x; if (i >= (size_t)S * D) return; int c = i % D; out[i] = f2bf(x1[i] + rbf(g[c] * bf2f(a[i]))); }

extern "C" void kernel_launch(void* const* d_in, const int* in_sizes, int n_in, void* d_out, int out_size, void* d_ws, size_t ws_size, hipStream_t stream) {
  const u16* x = (const u16*)d_in[0]; const u16* mem0 = (const u16*)d_in[1]; const u16* Wq = (const u16*)d_in[2]; const u16* Wk = (const u16*)d_in[3]; const u16* Wv = (const u16*)d_in[4]; const u16* Wo = (const u16*)d_in[5];
  const u16* wg = (const u16*)d_in[8]; const u16* wu = (const u16*)d_in[9]; const u16* wd = (const u16*)d_in[10]; const u16* c = (const u16*)d_in[11]; const u16* wm = (const u16*)d_in[12];
  char* ws = (char*)d_ws; size_t off = 0; auto alloc = [&](size_t b) { void* p = ws + off; off += (b + 255) & ~(size_t)255; return p; };
  float* mod = (float*)alloc(6 * D * 4); u16* h1 = (u16*)alloc((size_t)S * D * 2);
  u16* Q = (u16*)alloc((size_t)S * HD * 2); u16* K = (u16*)alloc((size_t)S * HD * 2); u16* V = (u16*)alloc((size_t)S * HD * 2); u16* O = (u16*)alloc((size_t)S * HD * 2);
  u16* qm = (u16*)alloc(M * HD * 2); u16* km = (u16*)alloc(M * HD * 2); u16* vm = (u16*)alloc(M * HD * 2); u16* om = (u16*)alloc(M * HD * 2); u16* mem = (u16*)alloc(M * D * 2);
  u16* A = (u16*)alloc((size_t)S * D * 2); float* x1 = (float*)alloc((size_t)S * D * 4); u16* h2 = (u16*)alloc((size_t)S * D * 2);
  u16* G = (u16*)alloc((size_t)S * F * 2); u16* U = (u16*)alloc((size_t)S * F * 2); u16* ACT = (u16*)alloc((size_t)S * F * 2); u16* Dn = (u16*)alloc((size_t)S * D * 2);
  k_mod<<<(6 * D + 255) / 256, 256, 0, stream>>>(c, wm, mod);
  k_ln<<<S, 256, 0, stream>>>(x, nullptr, mod + 0 * D, mod + 1 * D, h1);
  dim3 b(16, 16);
  k_gemm<<<dim3(HD / 16, S / 16), b, 0, stream>>>(h1, Wq, Q, S, HD, D, D, HD, HD);
  k_gemm<<<dim3(HD / 16, S / 16), b, 0, stream>>>(h1, Wk, K, S, HD, D, D, HD, HD);
  k_gemm<<<dim3(HD / 16, S / 16), b, 0, stream>>>(h1, Wv, V, S, HD, D, D, HD, HD);
  const u16* cur = mem0;
  for (int s = 0; s < NSEG; ++s) {
    k_gemm<<<dim3(HD / 16, 1), b, 0, stream>>>(cur, Wq, qm, M, HD, D, D, HD, HD);
    k_gemm<<<dim3(HD / 16, 1), b, 0, stream>>>(cur, Wk, km, M, HD, D, D, HD, HD);
    k_gemm<<<dim3(HD / 16, 1), b, 0, stream>>>(cur, Wv, vm, M, HD, D, D, HD, HD);
    size_t o = (size_t)s * SEG * HD;
    k_attn<<<dim3(H, L), 128, 0, stream>>>(qm, km, vm, Q + o, K + o, V + o, om, O + o);
    k_gemm<<<dim3(D / 16, 1), b, 0, stream>>>(om, Wo, mem, M, D, HD, HD, D, D);
    cur = mem;
  }
  k_gemm<<<dim3(D / 16, S / 16), b, 0, stream>>>(O, Wo, A, S, D, HD, HD, D, D);
  k_res<<<(S * D + 255) / 256, 256, 0, stream>>>(x, A, mod + 2 * D, x1);
  k_ln<<<S, 256, 0, stream>>>(nullptr, x1, mod + 3 * D, mod + 4 * D, h2);
  k_gemm<<<dim3(F / 16, S / 16), b, 0, stream>>>(h2, wg, G, S, F, D, D, F, F);
  k_gemm<<<dim3(F / 16, S / 16), b, 0, stream>>>(h2, wu, U, S, F, D, D, F, F);
  k_act<<<(int)(((size_t)S * F + 255) / 256), 256, 0, stream>>>(G, U, ACT);
  k_gemm<<<dim3(D / 16, S / 16), b, 0, stream>>>(ACT, wd, Dn, S, D, F, F, D, D);
  k_res2<<<(S * D + 255) / 256, 256, 0, stream>>>(x1, Dn, mod + 5 * D, (u16*)d_out);
}
```

```cpp
#include <hip/hip_runtime.h>
#include <hip/hip_bf16.h>

#define STAMPS 0
typedef unsigned short u16;
typedef unsigned v4u __attribute__((ext_vector_type(4)));
typedef unsigned v2u __attribute__((ext_vector_type(2)));
typedef unsigned v8u __attribute__((ext_vector_type(8)));
typedef short s8v __attribute__((ext_vector_type(8)));
typedef short s4v __attribute__((ext_vector_type(4)));
typedef float f4 __attribute__((ext_vector_type(4)));
typedef float f2 __attribute__((ext_vector_type(2)));
typedef __bf16 b2v __attribute__((ext_vector_type(2)));
typedef __bf16 b8v __attribute__((ext_vector_type(8)));
typedef __bf16 b16v __attribute__((ext_vector_type(16)));
#define LAS __attribute__((address_space(3)))
#define GAS __attribute__((address_space(1)))
#define RLX_AGENT __ATOMIC_RELAXED, __HIP_MEMORY_SCOPE_AGENT
typedef __amdgpu_buffer_rsrc_t rsrc_t;

constexpr int S_ = 2048, D_ = 576, HD_ = 4096, NSEG = 64, F_ = 4608;
constexpr int KS_D = 18, KS_F = 144;
constexpr unsigned SENT = 0xFFFFFFFFu;
constexpr unsigned OOB = 0x80000000u;

__device__ __forceinline__ float bflo(unsigned v) { return __uint_as_float(v << 16); }
__device__ __forceinline__ float bfhi(unsigned v) { return __uint_as_float(v & 0xFFFF0000u); }
__device__ __forceinline__ unsigned pack2(float lo, float hi) { f2 x = {lo, hi}; b2v y = __builtin_convertvector(x, b2v); return __builtin_bit_cast(unsigned, y); }
__device__ __forceinline__ float rbf(float f) { return bflo(pack2(f, 0.f)); }
__device__ __forceinline__ f4 mfma32(v4u a, v4u b, f4 c) { return __builtin_amdgcn_mfma_f32_16x16x32_bf16(__builtin_bit_cast(s8v, a), __builtin_bit_cast(s8v, b), c, 0, 0, 0); }
__device__ __forceinline__ f4 smf(v4u a, v8u b, f4 c, int idx) { return __builtin_amdgcn_smfmac_f32_16x16x64_bf16(__builtin_bit_cast(b8v, a), __builtin_bit_cast(b16v, b), c, idx, 0, 0); }
__device__ __forceinline__ v2u trread(LAS const void* p) { return __builtin_bit_cast(v2u, __builtin_amdgcn_ds_read_tr16_b64_v4i16((LAS s4v*)p)); }
__device__ __forceinline__ v4u cat(v2u a, v2u b) { v4u r; r.x = a.x; r.y = a.y; r.z = b.x; r.w = b.y; return r; }
__device__ __forceinline__ v8u cat8(v2u a, v2u b, v2u c, v2u d) { v8u r; r[0] = a.x; r[1] = a.y; r[2] = b.x; r[3] = b.y; r[4] = c.x; r[5] = c.y; r[6] = d.x; r[7] = d.y; return r; }
__device__ __forceinline__ v2u pack4(f4 a) { v2u r; r.x = pack2(a[0], a[1]); r.y = pack2(a[2], a[3]); return r; }
__device__ __forceinline__ v4u f4bits(f4 a) { return __builtin_bit_cast(v4u, a); }
__device__ __forceinline__ f4 bitsf4(v4u a) { return __builtin_bit_cast(f4, a); }
template <int CTRL> __device__ __forceinline__ float dppf(float v) { return __int_as_float(__builtin_amdgcn_update_dpp(0, __float_as_int(v), CTRL, 0xf, 0xf, true)); }
template <int N> __device__ __forceinline__ void waitv() { asm volatile("s_waitcnt vmcnt(%0)" :: "n"(N) : "memory"); }
#define SB() __builtin_amdgcn_sched_barrier(0)
#define RAWBAR() do { SB(); asm volatile("" ::: "memory"); __builtin_amdgcn_s_barrier(); asm volatile("" ::: "memory"); SB(); } while (0)
#define LDSBAR() do { asm volatile("s_waitcnt lgkmcnt(0)" ::: "memory"); __builtin_amdgcn_s_barrier(); asm volatile("" ::: "memory"); } while (0)
__device__ __forceinline__ void glds_s(const void* sbase, unsigned voff, unsigned lds_dst) {
  unsigned long long tmp;
  asm volatile("s_mov_b32 m0, %3\n\ts_mov_b64 %0, %2\n\tglobal_load_lds_dwordx4 %1, %0" : "=&s"(tmp) : "v"(voff), "s"(sbase), "s"(lds_dst));
}
#define glds_f glds_s
__device__ __forceinline__ void st16(rsrc_t r, unsigned off, v4u v) { __builtin_amdgcn_raw_buffer_store_b128(v, r, off, 0, 16); }
__device__ __forceinline__ void st8(rsrc_t r, unsigned off, v2u v) { __builtin_amdgcn_raw_buffer_store_b64(v, r, off, 0, 16); }
__device__ __forceinline__ v4u ld16(rsrc_t r, unsigned off, unsigned so) { return __builtin_amdgcn_raw_buffer_load_b128(r, off, so, 16); }
__device__ __forceinline__ unsigned umax3(unsigned a, unsigned b, unsigned c) { return max(max(a, b), c); }
__device__ __forceinline__ unsigned vmax4(v4u v) { return max(max(v.x, v.y), max(v.z, v.w)); }
__device__ __forceinline__ unsigned fixs(unsigned v) { return min(v, 0xFFFFFFFEu); }
__device__ __forceinline__ float g_max(float v) {
  float t;
  asm volatile("v_mov_b32 %1, %0\n\ts_nop 1\n\tv_permlane16_swap_b32 %0, %1\n\tv_max_f32 %0, %0, %1\n\tv_mov_b32 %1, %0\n\ts_nop 1\n\tv_permlane32_swap_b32 %0, %1\n\tv_max_f32 %0, %0, %1" : "+v"(v), "=&v"(t));
  return v;
}
__device__ __forceinline__ float g_sum(float v) {
  { const auto t = __builtin_amdgcn_permlane16_swap(__float_as_uint(v), __float_as_uint(v), false, false); v = __uint_as_float(t[0]) + __uint_as_float(t[1]); }
  { const auto t = __builtin_amdgcn_permlane32_swap(__float_as_uint(v), __float_as_uint(v), false, false); v = __uint_as_float(t[0]) + __uint_as_float(t[1]); }
  return v;
}
__device__ __forceinline__ float sw32add(float a, float b) { const auto x = __builtin_amdgcn_permlane32_swap(__float_as_uint(a), __float_as_uint(b), false, false); return __uint_as_float(x[0]) + __uint_as_float(x[1]); }
__device__ __forceinline__ unsigned now32() { unsigned long long t; asm volatile("s_memtime %0\n\ts_waitcnt lgkmcnt(0)" : "=s"(t) :: "memory"); return (unsigned)t; }
__device__ __forceinline__ void wait_until(unsigned t) {
  int rem = (int)(t - now32());
  while (rem > 200) { __builtin_amdgcn_s_sleep(2); rem -= 150; }
  while (rem > 0) rem = (int)(t - now32()) - 30;
}

constexpr size_t al(size_t x) { return (x + 4095) & ~(size_t)4095; }
constexpr size_t O_CTL = 0;
constexpr int C_TMO = 0, C_POL = 2, C_LN1 = 8, C_A = 24, C_PROG = 88, C_B1A = 152, C_B1B = 216, C_LN2 = 280, C_G1 = 304, C_G2 = 328, C_QH = 352, C_N = 360;
constexpr size_t O_MOD = al(O_CTL + C_N * 128);
constexpr size_t O_H1 = al(O_MOD + 6 * D_ * 4);
constexpr size_t O_Q = al(O_H1 + (size_t)S_ * D_ * 2);
constexpr size_t O_K = al(O_Q + (size_t)S_ * HD_ * 2);
constexpr size_t O_VF = al(O_K + (size_t)S_ * HD_ * 2);
constexpr size_t O_KM = al(O_VF + (size_t)NSEG * 36 * 32 * 1024);
constexpr size_t O_PF = al(O_KM + (size_t)NSEG * 32 * 2048);
constexpr size_t O_X1 = al(O_PF + (size_t)NSEG * 2 * 40 * 1024);
constexpr size_t O_H2 = al(O_X1 + (size_t)S_ * D_ * 4);
constexpr size_t O_ACT = al(O_H2 + (size_t)S_ * D_ * 2);
constexpr size_t O_SLAB = al(O_ACT + (size_t)S_ * F_ * 2);
constexpr size_t O_DBG = al(O_SLAB + (size_t)19456 * D_ * 4);
constexpr size_t O_XCH = al(O_DBG + 1048576);
constexpr int MEM_SLOT = 131072, MEM_REP = 32768;
constexpr int P_SLOT = 262144, P_REP = 65536;
constexpr size_t X_MEM = 0, X_P = (size_t)NSEG * MEM_SLOT, X_VM = X_P + (size_t)NSEG * P_SLOT, X_END = X_VM + (size_t)22 * 2097152;
constexpr size_t O_END = O_XCH + 2097152 + X_END;
static_assert(O_END <= ((size_t)256 << 20), "workspace");
__device__ __forceinline__ unsigned char* xch_base(unsigned char* ws) { return (unsigned char*)(((size_t)(ws + O_XCH) + 2097151) & ~(size_t)2097151); }
__device__ __forceinline__ unsigned vm_slot(int s) { return (unsigned)(s / 3) * 2097152u + (unsigned)(s % 3) * 589824u; }
__device__ __forceinline__ unsigned lmap(unsigned o) { return ((o >> 13) << 14) | (o & 8191u); }
__device__ __forceinline__ unsigned half_off(const unsigned char* slot, int pol) { return (((unsigned)pol ^ (unsigned)((size_t)slot >> 21)) & 1u) << 13; }
__device__ __forceinline__ GAS unsigned* ctl(unsigned char* ws, int i) { return (GAS unsigned*)(ws + O_CTL) + i * 32; }

constexpr int SPIN_CAP = 1 << 20;
__device__ __forceinline__ bool spin_fail(int& sp, GAS unsigned* tmo, unsigned code) {
  if (++sp > SPIN_CAP) { __hip_atomic_store(tmo, code, RLX_AGENT); return true; }
  if ((sp & 255) == 0 && __hip_atomic_load(tmo, RLX_AGENT) != 0u) return true;
  return false;
}

struct Args { const u16 *x, *mem0, *Wq, *Wk, *Wv, *Wo, *Wg, *Wu, *Wd, *c, *wm; u16* out; unsigned char* ws; int x1, x2, k0, k1; };

__global__ void __launch_bounds__(256) k_pre(Args a) {
  const int b = blockIdx.x, t = threadIdx.x;
  unsigned char* ws = a.ws;
  if (b < 54) {
    __shared__ float red[32][65];
    const int cg = t & 7, ksl = t >> 3;
    v4u w[18];
#pragma unroll
    for (int i = 0; i < 18; ++i) w[i] = *(const v4u*)(a.wm + (size_t)(ksl * 18 + i) * 3456 + b * 64 + cg * 8);
    float s[8] = {0, 0, 0, 0, 0, 0, 0, 0};
#pragma unroll
    for (int i = 0; i < 18; ++i) {
      const float cv = bflo(a.c[ksl * 18 + i]);
      const float sv = rbf(cv / (1.f + __expf(-cv)));
      s[0] += sv * bflo(w[i].x); s[1] += sv * bfhi(w[i].x); s[2] += sv * bflo(w[i].y); s[3] += sv * bfhi(w[i].y);
      s[4] += sv * bflo(w[i].z); s[5] += sv * bfhi(w[i].z); s[6] += sv * bflo(w[i].w); s[7] += sv * bfhi(w[i].w);
    }
#pragma unroll
    for (int j = 0; j < 8; ++j) red[ksl][cg * 8 + j] = s[j];
    __syncthreads();
    if (t < 64) { float v = 0; for (int i = 0; i < 32; ++i) v += red[i][t]; ((float*)(ws + O_MOD))[b * 64 + t] = rbf(v); }
    return;
  }
  unsigned char* xb = xch_base(ws);
  const rsrc_t rx = __builtin_amdgcn_make_buffer_rsrc(xb, 0, (int)X_END, 0x00020000);
  if (b == 54) {
    for (int i = t; i < C_N; i += 256) *ctl(ws, i) = 0u;
    for (int u = t; u < 9 * 64; u += 256) {
      const int cj = u >> 4, rh = (u >> 3) & 1, hf = (u >> 2) & 1, ii = u & 3;
      v4u v = *(const v4u*)(a.mem0 + (4 * rh + ii) * D_ + 16 * cj + 8 * hf);
      v.x = fixs(v.x); v.y = fixs(v.y); v.z = fixs(v.z); v.w = fixs(v.w);
#pragma unroll
      for (int r = 0; r < 6; ++r) st16(rx, (unsigned)X_MEM + (r >> 1) * MEM_REP + (r & 1) * 8192 + lmap(u * 16), v);
    }
    return;
  }
  const v4u p = {SENT, SENT, SENT, SENT};
  const unsigned nb = gridDim.x - 55, i0 = (b - 55) * 256 + t;
  for (unsigned u = i0; u < 2u * MEM_SLOT / 16; u += nb * 256) st16(rx, (unsigned)X_MEM + MEM_SLOT + u * 16, p);
  for (unsigned u = i0; u < 2u * P_SLOT / 16; u += nb * 256) st16(rx, (unsigned)X_P + u * 16, p);
  for (unsigned u = i0; u < 2u * 589824 / 16; u += nb * 256) st16(rx, (unsigned)X_VM + u * 16, p);
}

__device__ __forceinline__ int tr_off(int lane, int ct) {
  const int g = lane >> 4, q = (lane & 15) >> 2, p = lane & 3;
  const int f = (2 * g + (q >> 1)) & 3;
  return (4 * g + q) * 128 + ((ct ^ f) << 5) + ((p >> 1) << 4) + ((p & 1) << 3);
}
__device__ __forceinline__ unsigned stage_src(int lane, int r0, int ld) {
  const int row = r0 + (lane >> 3), p16 = lane & 7;
  return (unsigned)(row * ld + (p16 ^ (((row >> 1) & 3) << 1)) * 8) * 2u;
}
template <int NXT, int NB, int TM, int TN, bool WA, int NST, int BSTR = 4096>
struct Gemm {
  static constexpr int XB = NXT * 1024, NP = NXT + 4 * NB, PP = (NP + 7) / 8, STAGE = PP * 8192;
  static_assert(PP <= TN && TN >= 3 && NST >= 4 && NST * STAGE <= 160 * 1024 && (NST - 1) * PP < 64, "shape");
  struct Src { const unsigned char* sb[PP]; unsigned st[PP]; unsigned vo[PP]; };
  template <class F> static __device__ __forceinline__ void setup(Src& S, int wave, int lane, const unsigned char* xbase, size_t xtile, int ld, F wblk) {
#pragma unroll
    for (int j = 0; j < PP; ++j) {
      int i = wave + 8 * j; if (i >= NP) i = NXT;
      if (i < NXT) { S.sb[j] = xbase + (size_t)i * xtile; S.st[j] = 1024; S.vo[j] = lane * 16; }
      else { S.sb[j] = (const unsigned char*)wblk((i - NXT) >> 2); S.st[j] = 64u * ld; S.vo[j] = stage_src(lane, 8 * ((i - NXT) & 3), ld); }
    }
  }
  template <bool ISSUE, int HALF>
  static __device__ __forceinline__ void step(LAS unsigned char* L, unsigned ldsw, const Src& S, const int (&wb)[4], int xb, f4 (&acc)[TN][TM], v4u (&xf)[TM], v4u (&xn)[TM], v4u& w0, v4u& w1,
                                              int t, int& bc) {
    const int bn = bc == NST - 1 ? 0 : bc + 1, bi = bc == 0 ? NST - 1 : bc - 1;
    LAS unsigned char* cur = L + bc * STAGE;
    LAS unsigned char* nxt = L + bn * STAGE;
#pragma unroll
    for (int n = 0; n < TN; ++n) {
      v4u wn;
      if (n + 2 < TN) wn = cat(trread(cur + wb[(n + 2) & 3] + ((n + 2) >> 2) * BSTR), trread(cur + wb[(n + 2) & 3] + ((n + 2) >> 2) * BSTR + 2048));
      else wn = cat(trread(nxt + wb[n + 2 - TN]), trread(nxt + wb[n + 2 - TN] + 2048));
      if (n == TN - 3) {
#pragma unroll
        for (int m = 0; m < TM; ++m) xn[m] = *(LAS v4u*)(nxt + xb + m * 1024);
      }
#pragma unroll
      for (int m = 0; m < TM; ++m) acc[n][m] = WA ? mfma32(w0, xf[m], acc[n][m]) : mfma32(xf[m], w0, acc[n][m]);
      const int j = HALF == 0 ? n : n - (TN - PP);
      if (ISSUE && j >= 0 && j < PP) glds_s(S.sb[j] + (size_t)(t + NST - 1) * S.st[j], S.vo[j], ldsw + bi * STAGE + 8192 * j);
      w0 = w1; w1 = wn;
      SB();
    }
    if (ISSUE) waitv<(NST - 3) * PP>(); else waitv<0>();
    __builtin_amdgcn_s_barrier();
    asm volatile("" ::: "memory");
    bc = bn;
  }
  template <int HALF>
  static __device__ __forceinline__ void loop(LAS unsigned char* L, unsigned ldsw, const Src& S, const int (&wb)[4], int xb, f4 (&acc)[TN][TM], int nk) {
    v4u xa[TM], xc[TM], w0, w1;
#pragma unroll
    for (int m = 0; m < TM; ++m) xa[m] = *(LAS v4u*)(L + xb + m * 1024);
    w0 = cat(trread(L + wb[0]), trread(L + wb[0] + 2048));
    w1 = cat(trread(L + wb[1]), trread(L + wb[1] + 2048));
    int t = 0, bc = 0;
#pragma unroll 1
    for (; t + NST < nk; t += 2) {
      step<true, HALF>(L, ldsw, S, wb, xb, acc, xa, xc, w0, w1, t, bc);
      step<true, HALF>(L, ldsw, S, wb, xb, acc, xc, xa, w0, w1, t + 1, bc);
    }
    if (t + NST - 1 < nk) {
      step<true, HALF>(L, ldsw, S, wb, xb, acc, xa, xc, w0, w1, t, bc);
      step<false, HALF>(L, ldsw, S, wb, xb, acc, xc, xa, w0, w1, t + 1, bc);
      t += 2;
    }
#pragma unroll 1
    for (; t < nk; t += 2) {
      step<false, HALF>(L, ldsw, S, wb, xb, acc, xa, xc, w0, w1, t, bc);
      step<false, HALF>(L, ldsw, S, wb, xb, acc, xc, xa, w0, w1, t + 1, bc);
    }
  }
  static __device__ __forceinline__ void run(unsigned char* lds, int wave, const Src& S, const int (&wb)[4], int xb, f4 (&acc)[TN][TM], int nk) {
    LAS unsigned char* L = (LAS unsigned char*)lds;
    const unsigned ldsw = (unsigned)(size_t)L + wave * 1024;
#pragma unroll
    for (int t = 0; t < NST - 1; ++t) {
      const int tc = t;
#pragma unroll
      for (int j = 0; j < PP; ++j) glds_s(S.sb[j] + (size_t)tc * S.st[j], S.vo[j], ldsw + t * STAGE + 8192 * j);
    }
    waitv<(NST - 3) * PP>();
    __builtin_amdgcn_s_barrier();
    asm volatile("" ::: "memory");
    if (wave < 4) loop<0>(L, ldsw, S, wb, xb, acc, nk); else loop<1>(L, ldsw, S, wb, xb, acc, nk);
  }
};

#define TIDS() int tid = threadIdx.x; asm volatile("" : "+v"(tid)); const int wave = __builtin_amdgcn_readfirstlane(tid >> 6), lane = tid & 63
__device__ __forceinline__ int tid_opaque() { int t = threadIdx.x; asm volatile("" : "+v"(t)); return t; }
__device__ __forceinline__ void dep_wait(unsigned char* ws, int c, unsigned want) {
  if (__builtin_amdgcn_readfirstlane(tid_opaque()) < 64) {
    int sp = 0;
    while (__builtin_amdgcn_readfirstlane(__hip_atomic_load(ctl(ws, c), RLX_AGENT)) < want) {
      if (spin_fail(sp, ctl(ws, C_TMO), 0x100u + c)) break;
      __builtin_amdgcn_s_sleep(8);
    }
    __builtin_amdgcn_fence(__ATOMIC_ACQUIRE, "agent");
    waitv<0>();
  }
  LDSBAR();
}
__device__ __forceinline__ void dep_post(unsigned char* ws, int c, int n) {
  waitv<0>();
  LDSBAR();
  const int t = tid_opaque();
  if (t < n) __hip_atomic_fetch_add(ctl(ws, c + t), 1u, RLX_AGENT);
  asm volatile("" ::: "memory");
}
__device__ __forceinline__ float half_sum(float v) {
  v += dppf<0x128>(v); v += dppf<0x124>(v); v += dppf<0x122>(v); v += dppf<0x121>(v);
  const auto t = __builtin_amdgcn_permlane16_swap(__float_as_uint(v), __float_as_uint(v), false, false);
  return __uint_as_float(t[0]) + __uint_as_float(t[1]);
}

template <bool F32IN, int NT>
__device__ __forceinline__ void t_ln(const Args& a, unsigned char* ws, rsrc_t rw, unsigned char* lds, int tt0) {
  TIDS();
  LAS unsigned char* L = (LAS unsigned char*)lds;
  const int hl = lane & 31, tok = wave * 2 + (lane >> 5);
  const float* shift = (const float*)(ws + O_MOD) + (F32IN ? 3 : 0) * D_;
  const float* scale = shift + D_;
  f4 v[NT][5], sc[5], sh[5];
#pragma unroll
  for (int i = 0; i < 5; ++i) {
    const int u = min(hl + 32 * i, 143);
#pragma unroll
    for (int nt = 0; nt < NT; ++nt) {
      const size_t e = (size_t)((tt0 + nt) * 16 + tok) * D_ + 4 * u;
      if (F32IN) v[nt][i] = *(const f4*)((const float*)(ws + O_X1) + e);
      else { const v2u p = *(const v2u*)(a.x + e); v[nt][i] = f4{bflo(p.x), bfhi(p.x), bflo(p.y), bfhi(p.y)}; }
    }
    sc[i] = *(const f4*)(scale + 4 * u); sh[i] = *(const f4*)(shift + 4 * u);
  }
  const float wl = hl < 16 ? 1.f : 0.f;
#pragma unroll
  for (int nt = 0; nt < NT; ++nt) {
    float s = 0;
#pragma unroll
    for (int i = 0; i < 5; ++i) { const float t = (v[nt][i][0] + v[nt][i][1]) + (v[nt][i][2] + v[nt][i][3]); s += i == 4 ? (hl < 16 ? t : 0.f) : t; }
    const float mean = half_sum(s) * (1.f / D_);
    float q = 0;
#pragma unroll
    for (int i = 0; i < 5; ++i) {
      const f4 d = v[nt][i] - mean;
      const float t = (d[0] * d[0] + d[1] * d[1]) + (d[2] * d[2] + d[3] * d[3]);
      q += i == 4 ? (hl < 16 ? t : 0.f) : t;
    }
    const float rs = rsqrtf(half_sum(q) * (1.f / D_) + 1e-5f);
#pragma unroll
    for (int i = 0; i < 5; ++i) {
      const int u = min(hl + 32 * i, 143);
      const f4 o = (v[nt][i] - mean) * rs * (sc[i] + 1.f) + sh[i];
      *(LAS v2u*)(L + nt * 18432 + (u >> 3) * 1024 + ((u & 3) * 16 + tok) * 16 + ((u >> 2) & 1) * 8) = pack4(o);
    }
  }
  (void)wl;
  LDSBAR();
#pragma unroll
  for (int i = 0; i < (NT * 1152 + 511) / 512; ++i) {
    const int c = tid + 512 * i;
    const v4u r = *(LAS v4u*)(L + min(c, NT * 1152 - 1) * 16);
    st16(rw, c < NT * 1152 ? (unsigned)(F32IN ? O_H2 : O_H1) + tt0 * 18432 + c * 16 : OOB, r);
  }
}

template <int TM>
__device__ __forceinline__ void t_a(const Args& a, unsigned char* ws, rsrc_t rw, unsigned char* lds, int s0, int h) {
  TIDS();
  typedef Gemm<2 * TM, 6, TM, 6, true, 4> G;
  LAS unsigned char* L = (LAS unsigned char*)lds;
  const int wm = wave >> 2, wn = wave & 3;
  constexpr int VOFF = 128 * 1024;
  const u16 *Wq = a.Wq, *Wk = a.Wk, *Wv = a.Wv, *Wo = a.Wo;
  {
    typename G::Src S;
    G::setup(S, wave, lane, ws + O_H1 + (size_t)s0 * 2 * KS_D * 1024, (size_t)KS_D * 1024, HD_,
             [&](int b) { return (b < 2 ? Wq : b < 4 ? Wk : Wv) + h * 128 + (b & 1) * 64; });
    int wb[4];
#pragma unroll
    for (int n = 0; n < 4; ++n) { const int c24 = 6 * wn + n; wb[n] = G::XB + (c24 >> 2) * 4096 + tr_off(lane, c24 & 3); }
    f4 acc[6][TM];
#pragma unroll
    for (int n = 0; n < 6; ++n)
#pragma unroll
      for (int m = 0; m < TM; ++m) acc[n][m] = f4{0, 0, 0, 0};
    G::run(lds, wave, S, wb, wm * TM * 1024 + lane * 16, acc, KS_D);
    const unsigned ldsw = (unsigned)(size_t)L + wave * 1024;
#pragma unroll
    for (int ps = 0; ps < 2; ++ps)
#pragma unroll
      for (int it = 0; it < 6; ++it) glds_s(Wo + (size_t)(h * 128 + 64 * (it & 1)) * D_ + (ps * 3 + (it >> 1)) * 64, stage_src(lane, 8 * wave, D_), ldsw + ps * 49152 + it * 8192);
    v4u fr[3][TM];
#pragma unroll
    for (int pn = 0; pn < 3; ++pn) {
      const int p12 = 3 * wn + pn, wp = p12 & 3;
#pragma unroll
      for (int m = 0; m < TM; ++m) {
        fr[pn][m] = cat(pack4(acc[2 * pn][m]), pack4(acc[2 * pn + 1][m]));
        if (p12 >= 8) *(LAS v4u*)(L + VOFF + ((wm * TM + m) * 4 + wp) * 1024 + lane * 16) = fr[pn][m];
      }
    }
    waitv<0>(); LDSBAR();
#pragma unroll
    for (int pn = 0; pn < 3; ++pn) {
      const int p12 = 3 * wn + pn, mat = p12 >> 2, wp = p12 & 3;
#pragma unroll
      for (int m = 0; m < TM; ++m) {
        const int T = s0 * 2 + wm * TM + m;
        if (mat < 2) st16(rw, (unsigned)(mat == 0 ? O_Q : O_K) + ((((T >> 1) * 32 + h) * 4 + wp) * 2 + (T & 1)) * 1024 + lane * 16, fr[pn][m]);
      }
    }
  }
  constexpr int M2 = TM == 1 ? 2 : TM;
  const int mt0 = TM == 1 ? 0 : wm * TM;
#pragma unroll 1
  for (int ps = 0; ps < 3; ++ps) {
    LAS unsigned char* W = L + (ps & 1) * 49152;
    f4 c[M2][3];
#pragma unroll
    for (int m = 0; m < M2; ++m)
#pragma unroll
      for (int n = 0; n < 3; ++n) c[m][n] = f4{0, 0, 0, 0};
    if (TM > 1 || wm == 0) {
#pragma unroll
      for (int ks = 0; ks < 4; ++ks) {
        v4u vf[M2];
#pragma unroll
        for (int m = 0; m < M2; ++m) vf[m] = *(LAS v4u*)(L + VOFF + ((mt0 + m) * 4 + ks) * 1024 + lane * 16);
#pragma unroll
        for (int n = 0; n < 3; ++n) {
          const int c12 = 3 * wn + n;
          const int o = (c12 >> 2) * 16384 + ks * 4096 + tr_off(lane, c12 & 3);
          const v4u wf = cat(trread(W + o), trread(W + o + 2048));
#pragma unroll
          for (int m = 0; m < M2; ++m) c[m][n] = mfma32(vf[m], wf, c[m][n]);
        }
      }
    }
    if (ps == 0) {
      LDSBAR();
      const unsigned ldsw = (unsigned)(size_t)L + wave * 1024;
#pragma unroll
      for (int it = 0; it < 6; ++it) glds_s(Wo + (size_t)(h * 128 + 64 * (it & 1)) * D_ + (6 + (it >> 1)) * 64, stage_src(lane, 8 * wave, D_), ldsw + it * 8192);
      asm volatile("" ::: "memory");
    }
    if (TM > 1 || wm == 0) {
#pragma unroll
      for (int sg = 0; sg < M2 / 2; ++sg) {
        const int s = s0 + (mt0 >> 1) + sg;
#pragma unroll
        for (int n = 0; n < 3; ++n) {
          const int cj = ps * 12 + 3 * wn + n;
          st16(rw, (unsigned)O_VF + ((s * 36 + cj) * 32 + (h & 28) + (lane >> 4)) * 1024 + ((h & 3) * 16 + (lane & 15)) * 16, cat(pack4(c[2 * sg][n]), pack4(c[2 * sg + 1][n])));
        }
      }
    }
    if (ps == 1) { if (TM > 1) waitv<3 * M2>(); else waitv<0>(); LDSBAR(); }
  }
}

__device__ __forceinline__ void t_b1a(const Args& a, unsigned char* ws, rsrc_t rw, int s, int hq, int nh) {
  TIDS();
  const int g = lane >> 4, tok = lane & 15;
#pragma unroll 1
  for (int hi = 0; hi < nh; ++hi) {
  const int h = (hq * nh + hi) * 8 + wave;
  const unsigned char* qb = ws + O_Q + ((size_t)s * 32 + h) * 8192 + lane * 16;
  const unsigned char* kb = ws + O_K + ((size_t)s * 32 + h) * 8192 + lane * 16;
  const unsigned char* mb = ws + O_KM + ((size_t)s * 32 + h) * 2048 + (g * 8 + (tok & 7)) * 16;
  f4 sm[2] = {{0, 0, 0, 0}, {0, 0, 0, 0}}, s0[2] = {{0, 0, 0, 0}, {0, 0, 0, 0}}, s1 = {0, 0, 0, 0};
#pragma unroll
  for (int wp = 0; wp < 4; ++wp) {
    const v4u q0 = *(const v4u*)(qb + wp * 2048), q1 = *(const v4u*)(qb + wp * 2048 + 1024);
    const v4u k0 = *(const v4u*)(kb + wp * 2048), k1 = *(const v4u*)(kb + wp * 2048 + 1024);
    const v4u km = *(const v4u*)(mb + wp * 512);
    sm[0] = mfma32(km, q0, sm[0]); sm[1] = mfma32(km, q1, sm[1]);
    s0[0] = mfma32(k0, q0, s0[0]); s0[1] = mfma32(k0, q1, s0[1]);
    s1 = mfma32(k1, q1, s1);
  }
  const float sc = 0.08838834764831845f * 1.4426950408889634f, NEG = -3e38f;
#pragma unroll
  for (int tt = 0; tt < 2; ++tt) {
    float p[4], b[4], c[4];
    float mx = NEG;
#pragma unroll
    for (int r = 0; r < 4; ++r) {
      const int key = 4 * g + r;
      p[r] = g < 2 ? sm[tt][r] * sc : NEG;
      b[r] = (tt == 1 || key <= tok) ? s0[tt][r] * sc : NEG;
      c[r] = (tt == 1 && key <= tok) ? s1[r] * sc : NEG;
      mx = fmaxf(mx, fmaxf(p[r], fmaxf(b[r], c[r])));
    }
    mx = g_max(mx);
    float z = 0;
#pragma unroll
    for (int r = 0; r < 4; ++r) {
      const int key = 4 * g + r;
      p[r] = g < 2 ? __builtin_amdgcn_exp2f(p[r] - mx) : 0.f;
      b[r] = (tt == 1 || key <= tok) ? __builtin_amdgcn_exp2f(b[r] - mx) : 0.f;
      c[r] = (tt == 1 && key <= tok) ? __builtin_amdgcn_exp2f(c[r] - mx) : 0.f;
      z += p[r] + b[r] + c[r];
    }
    const float iz = __builtin_amdgcn_rcpf(g_sum(z));
    v4u o; o.x = pack2(b[0] * iz, b[1] * iz); o.y = pack2(b[2] * iz, b[3] * iz); o.z = pack2(c[0] * iz, c[1] * iz); o.w = pack2(c[2] * iz, c[3] * iz);
    const unsigned pb = (unsigned)O_PF + (s * 2 + tt) * 40960;
    st16(rw, pb + ((h & 28) + g) * 1024 + ((h & 3) * 16 + tok) * 16, o);
    v2u m; m.x = pack2(p[0] * iz, p[1] * iz); m.y = pack2(p[2] * iz, p[3] * iz);
    st8(rw, g < 2 ? pb + (32 + (h >> 2)) * 1024 + ((h & 3) * 16 + tok) * 16 + g * 8 : OOB, m);
  }
  }
}

template <int NCT>
__device__ __forceinline__ void t_b1b(const Args& a, unsigned char* ws, rsrc_t rw, unsigned char* lds, int s, int part, int pol) {
  TIDS();
  const int g = lane >> 4, tok = lane & 15;
  const int cg = wave & 3, kh = wave >> 2, cj0 = (part * 4 + cg) * NCT;
  LAS unsigned char* L = (LAS unsigned char*)lds;
  const unsigned ldsw = (unsigned)(size_t)L + wave * 1024;
#pragma unroll
  for (int it = 0; it < 10; ++it) glds_s(ws + O_PF + (size_t)s * 81920 + (wave + 8 * it) * 1024, lane * 16, ldsw + it * 8192);
  asm volatile("" ::: "memory");
  const unsigned char* vm0 = xch_base(ws) + X_VM + vm_slot(s);
  const unsigned char* vmb = ws + (size_t)(vm0 - ws) + half_off(vm0, pol) + (size_t)cj0 * 16384 + kh * 4096 + lane * 16;
  const unsigned char* vfb = ws + O_VF + (((size_t)s * 36 + cj0) * 32 + kh * 16) * 1024 + lane * 16;
  v4u av[2][20];
#define B1LD(n) do { _Pragma("unroll") for (int i = 0; i < 16; ++i) av[(n) & 1][i] = *(const v4u*)(vfb + (n) * 32768 + i * 1024); \
                     _Pragma("unroll") for (int i = 0; i < 4; ++i) av[(n) & 1][16 + i] = *(const v4u*)(vmb + (n) * 16384 + i * 1024); } while (0)
  B1LD(0);
  asm volatile("s_waitcnt vmcnt(20)" ::: "memory");
  LDSBAR();
  f4 acc[NCT][2];
#pragma unroll
  for (int n = 0; n < NCT; ++n) {
    if (n + 1 < NCT) B1LD(n + 1);
    acc[n][0] = f4{0, 0, 0, 0}; acc[n][1] = f4{0, 0, 0, 0};
#pragma unroll
    for (int i = 0; i < 20; ++i) {
      const int ks = i < 16 ? kh * 16 + i : 32 + kh * 4 + (i - 16);
      const v4u p0 = *(LAS v4u*)(L + ks * 1024 + lane * 16), p1 = *(LAS v4u*)(L + 40960 + ks * 1024 + lane * 16);
      acc[n][0] = mfma32(av[n & 1][i], p0, acc[n][0]); acc[n][1] = mfma32(av[n & 1][i], p1, acc[n][1]);
    }
    *(LAS f4*)(L + 81920 + (n * 8 + wave) * 1024 + lane * 16) = kh == 0 ? acc[n][1] : acc[n][0];
  }
  LDSBAR();
#pragma unroll
  for (int n = 0; n < NCT; ++n) {
    const f4 o = (kh == 0 ? acc[n][0] : acc[n][1]) + *(LAS f4*)(L + 81920 + (n * 8 + (wave ^ 4)) * 1024 + lane * 16);
    const int col = (cj0 + n) * 16 + 4 * g;
    const unsigned row = (unsigned)(s * 32 + kh * 16 + tok) * D_;
    const v2u xv = *(const v2u*)(a.x + row + col);
    const f4 gv = *(const f4*)((const float*)(ws + O_MOD) + 2 * D_ + col);
    f4 r; r[0] = bflo(xv.x) + gv[0] * o[0]; r[1] = bfhi(xv.x) + gv[1] * o[1]; r[2] = bflo(xv.y) + gv[2] * o[2]; r[3] = bfhi(xv.y) + gv[3] * o[3];
    st16(rw, (unsigned)O_X1 + (row + col) * 4, f4bits(r));
  }
}

__device__ __forceinline__ float gelu_t(float x) {
  const float w = x * (-2.3022082f - 0.10294324f * x * x);
  return x * __builtin_amdgcn_rcpf(1.f + __builtin_amdgcn_exp2f(w));
}
template <int TM>
__device__ __forceinline__ void t_g1(const Args& a, unsigned char* ws, rsrc_t rw, unsigned char* lds, int s0, int fb) {
  TIDS();
  typedef Gemm<2 * TM, 4, TM, 4, true, 6> G;
  const int wm = wave >> 2, wn = wave & 3;
  const u16 *Wg = a.Wg, *Wu = a.Wu;
  typename G::Src S;
  G::setup(S, wave, lane, ws + O_H2 + (size_t)s0 * 2 * KS_D * 1024, (size_t)KS_D * 1024, F_, [&](int b) { return (b < 2 ? Wg : Wu) + fb * 128 + (b & 1) * 64; });
  int wb[4];
#pragma unroll
  for (int n = 0; n < 4; ++n) wb[n] = G::XB + ((n >> 1) * 2 + (wn >> 1)) * 4096 + tr_off(lane, (wn & 1) * 2 + (n & 1));
  f4 acc[4][TM];
#pragma unroll
  for (int n = 0; n < 4; ++n)
#pragma unroll
    for (int m = 0; m < TM; ++m) acc[n][m] = f4{0, 0, 0, 0};
  G::run(lds, wave, S, wb, wm * TM * 1024 + lane * 16, acc, KS_D);
#pragma unroll
  for (int m = 0; m < TM; ++m) {
    f4 p, q;
#pragma unroll
    for (int r = 0; r < 4; ++r) { p[r] = gelu_t(acc[0][m][r]) * acc[2][m][r]; q[r] = gelu_t(acc[1][m][r]) * acc[3][m][r]; }
    const int T = s0 * 2 + wm * TM + m, ks = fb * 4 + wn;
    st16(rw, (unsigned)O_ACT + (T * KS_F + ks) * 1024 + lane * 16, cat(pack4(p), pack4(q)));
  }
}

__device__ __forceinline__ void t_g1x(const Args& a, unsigned char* ws, rsrc_t rw, unsigned char* lds, int s0, int fb) {
  TIDS();
  typedef Gemm<16, 4, 4, 8, true, 5, 8192> G;
  const int wm = wave >> 1, wn = wave & 1;
  const u16 *Wg = a.Wg, *Wu = a.Wu;
  G::Src S;
  G::setup(S, wave, lane, ws + O_H2 + (size_t)s0 * 2 * KS_D * 1024, (size_t)KS_D * 1024, F_, [&](int b) { return (b < 2 ? Wg : Wu) + fb * 128 + (b & 1) * 64; });
  int wb[4];
#pragma unroll
  for (int n = 0; n < 4; ++n) wb[n] = G::XB + wn * 4096 + tr_off(lane, n);
  f4 acc[8][4];
#pragma unroll
  for (int n = 0; n < 8; ++n)
#pragma unroll
    for (int m = 0; m < 4; ++m) acc[n][m] = f4{0, 0, 0, 0};
  G::run(lds, wave, S, wb, wm * 4096 + lane * 16, acc, KS_D);
#pragma unroll
  for (int pp = 0; pp < 2; ++pp)
#pragma unroll
    for (int m = 0; m < 4; ++m) {
      f4 p, q;
#pragma unroll
      for (int r = 0; r < 4; ++r) { p[r] = gelu_t(acc[2 * pp][m][r]) * acc[4 + 2 * pp][m][r]; q[r] = gelu_t(acc[2 * pp + 1][m][r]) * acc[5 + 2 * pp][m][r]; }
      const int T = s0 * 2 + wm * 4 + m, ks = fb * 4 + wn * 2 + pp;
      st16(rw, (unsigned)O_ACT + (T * KS_F + ks) * 1024 + lane * 16, cat(pack4(p), pack4(q)));
    }
}

template <int TM, int NB>
__device__ __forceinline__ void t_g2(const Args& a, unsigned char* ws, rsrc_t rw, unsigned char* lds, int s0, int k0, int nk, int srow) {
  TIDS();
  typedef Gemm<2 * TM, NB, TM, NB, true, 5> G;
  constexpr int b0 = NB == 5 ? 0 : 5;
  const int wm = wave >> 2, wn = wave & 3, g = lane >> 4, tok = lane & 15;
  const u16* Wd = a.Wd;
  typename G::Src S;
  G::setup(S, wave, lane, ws + O_ACT + ((size_t)s0 * 2 * KS_F + k0) * 1024, (size_t)KS_F * 1024, D_, [&](int b) { return Wd + (size_t)k0 * 32 * D_ + (b0 + b) * 64; });
  int wb[4];
#pragma unroll
  for (int n = 0; n < 4; ++n) { const int cj = NB * wn + n; wb[n] = G::XB + (cj >> 2) * 4096 + tr_off(lane, cj & 3); }
  f4 acc[NB][TM];
#pragma unroll
  for (int n = 0; n < NB; ++n)
#pragma unroll
    for (int m = 0; m < TM; ++m) acc[n][m] = f4{0, 0, 0, 0};
  G::run(lds, wave, S, wb, wm * TM * 1024 + lane * 16, acc, nk);
#pragma unroll
  for (int n = 0; n < NB; ++n)
#pragma unroll
    for (int m = 0; m < TM; ++m)
      st16(rw, (unsigned)O_SLAB + (unsigned)((srow + (wm * TM + m) * 16 + tok) * D_ + b0 * 64 + (NB * wn + n) * 16 + 4 * g) * 4, f4bits(acc[n][m]));
}

template <int NB>
__device__ __forceinline__ void t_g2x(const Args& a, unsigned char* ws, rsrc_t rw, unsigned char* lds, int s0, int k0, int nk, int srow) {
  TIDS();
  typedef Gemm<16, NB, 4, 2 * NB, true, 4> G;
  constexpr int b0 = NB == 5 ? 0 : 5;
  const int wm = wave >> 1, wn = wave & 1, g = lane >> 4, tok = lane & 15;
  const u16* Wd = a.Wd;
  typename G::Src S;
  G::setup(S, wave, lane, ws + O_ACT + ((size_t)s0 * 2 * KS_F + k0) * 1024, (size_t)KS_F * 1024, D_, [&](int b) { return Wd + (size_t)k0 * 32 * D_ + (b0 + b) * 64; });
  int wb[4];
#pragma unroll
  for (int n = 0; n < 4; ++n) { const int cj = 2 * NB * wn + n; wb[n] = G::XB + (cj >> 2) * 4096 + tr_off(lane, cj & 3); }
  f4 acc[2 * NB][4];
#pragma unroll
  for (int n = 0; n < 2 * NB; ++n)
#pragma unroll
    for (int m = 0; m < 4; ++m) acc[n][m] = f4{0, 0, 0, 0};
  G::run(lds, wave, S, wb, wm * 4096 + lane * 16, acc, nk);
#pragma unroll
  for (int n = 0; n < 2 * NB; ++n)
#pragma unroll
    for (int m = 0; m < 4; ++m)
      st16(rw, (unsigned)O_SLAB + (unsigned)((srow + (wm * 4 + m) * 16 + tok) * D_ + b0 * 64 + (2 * NB * wn + n) * 16 + 4 * g) * 4, f4bits(acc[n][m]));
}

template <int NSL>
__device__ __forceinline__ void t_fin(const Args& a, unsigned char* ws, int tok0, int srow, int ntok, int nit) {
  TIDS();
  const float* gb = (const float*)(ws + O_MOD) + 5 * D_;
  const int cmax = nit == 3 ? 1151 : nit * 512 - 1;
#pragma unroll 3
  for (int it = 0; it < nit; ++it) {
    const int c = min(it * 512 + tid, cmax);
    const float* sl = (const float*)(ws + O_SLAB) + (size_t)srow * D_ + c * 4;
    f4 v[NSL];
#pragma unroll
    for (int j = 0; j < NSL; ++j) v[j] = *(const f4*)(sl + (size_t)j * ntok * D_);
    const unsigned i = (unsigned)tok0 * D_ + c * 4;
    const f4 x1 = *(const f4*)((const float*)(ws + O_X1) + i);
    const f4 gv = *(const f4*)(gb + (c % 144) * 4);
    f4 acc = {0, 0, 0, 0};
#pragma unroll
    for (int j = 0; j < NSL; j += 4) acc += (v[j] + v[j + 1]) + (v[j + 2] + v[j + 3]);
    *(v2u*)(a.out + i) = pack4(x1 + gv * acc);
  }
}

enum { T_LN1, T_AS, T_AB, T_B1A, T_B1B, T_G1, T_G2, T_FIN, T_AM };
constexpr int NUX = 5, NU0 = 3, NU1 = 4, SEG0 = 8 * NUX, SEG1 = SEG0 + 4 * NU0, SEG2 = SEG1 + 2 * NU1, B1F = 56;
__device__ __forceinline__ int unit_of(int s) { return s < SEG0 ? s >> 3 : s < SEG1 ? NUX + ((s - SEG0) >> 2) : s < SEG2 ? NUX + NU0 + ((s - SEG1) >> 1) : NUX + NU0 + NU1 + s - SEG2; }
constexpr int Q_END = 2248;
__device__ const unsigned QOFF[9] = {0,202,408,609,812,1174,1531,1890,2248};
__device__ const unsigned QT[Q_END] = {
  128u,320u,512u,768u,960u,1152u,1344u,1600u,1792u,1984u,1u,4097u,17u,4113u,33u,4129u,
  49u,4145u,72u,4168u,104u,4200u,130u,4226u,194u,4290u,258u,4354u,322u,4418u,3u,35u,
  386u,4482u,8196u,67u,4163u,8228u,99u,4195u,450u,4546u,8260u,131u,4227u,8292u,514u,4610u,
  179u,195u,211u,4323u,578u,4674u,4292u,5u,4101u,244u,642u,4738u,4356u,8484u,706u,4802u,
  371u,134217734u,134221830u,340u,133u,4229u,8564u,770u,4866u,8580u,4563u,834u,4930u,452u,134217862u,134221958u,
  515u,4627u,261u,4357u,547u,4643u,898u,4994u,8708u,16391u,611u,962u,5058u,4676u,134217990u,134222086u,
  4708u,389u,4485u,675u,4787u,135u,723u,755u,134218118u,134222214u,8916u,517u,4613u,8948u,835u,16647u,
  645u,4741u,4916u,134218246u,134222342u,852u,709u,4805u,134218374u,134222470u,900u,17284u,931u,21396u,25508u,948u,
  979u,773u,4869u,134218438u,134222534u,964u,29636u,901u,4997u,9172u,25556u,5107u,837u,4933u,5092u,21476u,
  647u,17031u,33415u,49799u,933u,5029u,1012u,17396u,33780u,869u,4965u,134218502u,134222598u,134218630u,134222726u,965u,
  5061u,134218566u,134222662u,981u,5077u,134218662u,134222758u,997u,5093u,134218598u,134222694u,134218694u,134222790u,12807u,29191u,21191u,
  37575u,53959u,1013u,5109u,134218710u,134222806u,4999u,21383u,4935u,21319u,134218726u,134222822u,5063u,134218742u,134222838u,5079u,
  5031u,21415u,4967u,21351u,5095u,4871u,21255u,37639u,54023u,5111u,144u,336u,528u,784u,976u,1168u,
  1360u,1616u,1808u,2000u,8193u,12289u,8209u,12305u,8225u,12321u,8241u,12337u,8264u,12360u,8296u,12392u,
  8322u,12418u,8386u,12482u,8450u,12546u,8514u,12610u,4099u,4131u,8578u,12674u,51u,4147u,8212u,83u,
  4179u,8244u,8642u,12738u,115u,4211u,8276u,147u,163u,8706u,12802u,132u,4307u,8770u,12866u,196u,
  259u,8197u,12293u,4340u,8834u,12930u,8452u,339u,4451u,8898u,12994u,8516u,136323078u,136327174u,403u,8325u,
  12421u,4468u,8962u,13058u,4484u,467u,4579u,9026u,13122u,8644u,136323206u,136327302u,531u,8453u,12549u,4596u,
  9090u,13186u,4612u,12295u,8756u,9154u,13250u,627u,136323334u,136327430u,4739u,8581u,12677u,628u,691u,4803u,
  28807u,4835u,136323462u,136327558u,4820u,8709u,12805u,4852u,4915u,12551u,8837u,12933u,820u,4979u,136323590u,136327686u,
  899u,5011u,8901u,12997u,136323718u,136327814u,13188u,29572u,916u,5043u,963u,20871u,13267u,8965u,13061u,136323782u,
  136327878u,13252u,5091u,13283u,9093u,13189u,5076u,21460u,1011u,9029u,13125u,996u,17380u,33764u,12935u,29319u,
  45703u,62087u,9125u,13221u,13300u,29684u,9061u,13157u,136323846u,136327942u,136323974u,136328070u,9157u,13253u,136323910u,136328006u,
  9173u,13269u,136324006u,136328102u,9189u,13285u,136323942u,136328038u,136324038u,136328134u,8711u,25095u,8903u,25287u,41671u,58055u,
  9205u,13301u,136324054u,136328150u,9095u,25479u,9031u,25415u,136324070u,136328166u,9159u,136324086u,136328182u,9175u,9127u,25511u,
  9063u,25447u,9191u,8967u,25351u,41735u,58119u,9207u,160u,352u,544u,800u,992u,1184u,1376u,1632u,
  1824u,2016u,16385u,20481u,16401u,20497u,16417u,20513u,16433u,20529u,16456u,20552u,16488u,20584u,16514u,20610u,
  16578u,20674u,16642u,20738u,16706u,20802u,19u,16770u,20866u,4u,20u,36u,52u,16834u,20930u,68u,
  84u,100u,16898u,20994u,4228u,4276u,16962u,21058u,243u,4355u,16389u,20485u,8436u,17026u,21122u,307u,
  4403u,323u,4435u,17090u,21186u,324u,138428422u,138432518u,4483u,16517u,20613u,372u,4515u,17154u,21250u,435u,
  451u,483u,17218u,21314u,4548u,138428550u,138432646u,4611u,16645u,20741u,500u,17282u,21378u,516u,579u,595u,
  4707u,17346u,21442u,8772u,138428678u,138432774u,659u,16773u,20869u,4724u,4756u,8884u,138428806u,138432902u,771u,4883u,
  16901u,20997u,803u,4899u,4931u,20743u,17029u,21125u,9012u,138428934u,138433030u,4948u,17093u,21189u,138429062u,138433158u,
  4996u,21380u,5027u,25492u,29604u,5044u,5075u,17157u,21253u,138429126u,138433222u,5060u,33732u,17285u,21381u,13268u,
  29652u,9203u,17221u,21317u,9188u,25572u,4743u,21127u,37511u,53895u,17317u,21413u,5108u,21492u,17253u,21349u,
  138429190u,138433286u,138429318u,138433414u,17349u,21445u,138429254u,138433350u,17365u,21461u,138429350u,138433446u,17381u,21477u,138429286u,138433382u,
  138429382u,138433478u,519u,16903u,711u,12999u,29383u,45767u,62151u,17397u,21493u,138429398u,138433494u,13191u,29575u,13127u,
  29511u,138429414u,138433510u,13255u,138429430u,138433526u,13271u,13223u,29607u,13159u,29543u,13287u,13063u,29447u,45831u,62215u,
  13303u,176u,368u,560u,816u,1008u,1200u,1392u,1648u,1840u,2032u,24577u,28673u,24593u,28689u,24609u,
  28705u,24625u,28721u,24648u,28744u,24680u,28776u,24706u,28802u,24770u,28866u,24834u,28930u,24898u,28994u,4115u,
  24962u,29058u,4100u,4116u,4132u,4148u,25026u,29122u,4164u,4180u,4196u,25090u,29186u,8324u,8372u,25154u,
  29250u,4339u,275u,24581u,28677u,291u,4387u,25218u,29314u,260u,4419u,355u,25282u,29378u,4420u,140533766u,
  140537862u,8548u,24709u,28805u,419u,25346u,29442u,388u,8612u,25410u,29506u,499u,140533894u,140537990u,468u,24837u,
  28933u,8692u,25474u,29570u,563u,4659u,4675u,4691u,25538u,29634u,580u,140534022u,140538118u,643u,4755u,24965u,
  29061u,8820u,707u,12423u,739u,4851u,140534150u,140538246u,4867u,25093u,29189u,756u,819u,263u,851u,25221u,
  29317u,867u,4963u,140534278u,140538374u,9044u,25285u,29381u,140534406u,140538502u,9092u,25476u,9123u,29588u,33700u,9140u,
  9171u,25349u,29445u,140534470u,140538566u,9156u,995u,9187u,25477u,29573u,980u,17364u,33748u,13299u,25413u,29509u,
  13284u,29668u,8839u,25223u,41607u,57991u,25509u,29605u,9204u,25588u,25445u,29541u,140534534u,140538630u,140534662u,140538758u,
  25541u,29637u,140534598u,140538694u,25557u,29653u,140534694u,140538790u,25573u,29669u,140534630u,140538726u,140534726u,140538822u,4615u,20999u,
  4807u,17095u,33479u,49863u,25589u,29685u,140534742u,140538838u,903u,17287u,839u,17223u,140534758u,140538854u,967u,140534774u,
  140538870u,983u,935u,17319u,871u,17255u,999u,775u,17159u,33543u,49927u,1015u,0u,64u,192u,256u,
  384u,448u,576u,640u,704u,832u,896u,1024u,1088u,1216u,1280u,1408u,1472u,1536u,1664u,1728u,
  1856u,1920u,32769u,36865u,40961u,45057u,49153u,53249u,32785u,36881u,40977u,45073u,49169u,53265u,32801u,36897u,
  40993u,45089u,49185u,53281u,32817u,36913u,41009u,45105u,49201u,53297u,32840u,36936u,41032u,45128u,49224u,53320u,
  32872u,36968u,41064u,45160u,49256u,53352u,32898u,36994u,41090u,45186u,49282u,53378u,32962u,37058u,41154u,45250u,
  49346u,53442u,33026u,37122u,41218u,45314u,49410u,53506u,33090u,37186u,41282u,45378u,49474u,53570u,33154u,37250u,
  41346u,45442u,49538u,53634u,33218u,37314u,41410u,45506u,49602u,53698u,4243u,4259u,33282u,37378u,41474u,45570u,
  49666u,53762u,4275u,4291u,164u,227u,33346u,37442u,41538u,45634u,49730u,53826u,8404u,4371u,32773u,36869u,
  40965u,45061u,49157u,53253u,57349u,33410u,37506u,41602u,45698u,49794u,53890u,276u,4388u,33474u,37570u,41666u,
  45762u,49858u,53954u,4467u,478183430u,478187526u,356u,32901u,36997u,41093u,45189u,49285u,53381u,57477u,33538u,37634u,
  41730u,45826u,49922u,54018u,404u,4516u,33602u,37698u,41794u,45890u,49986u,54082u,4595u,478183558u,478187654u,484u,
  33029u,37125u,41221u,45317u,49413u,53509u,57605u,33666u,37762u,41858u,45954u,50050u,54146u,4628u,8199u,548u,
  4660u,33730u,37826u,41922u,46018u,50114u,54210u,478183686u,478187782u,612u,33157u,37253u,41349u,45445u,49541u,53637u,
  57733u,8836u,8327u,24711u,4819u,692u,8900u,478183814u,478187910u,8932u,33285u,37381u,41477u,45573u,49669u,53765u,
  57861u,788u,8455u,4900u,33413u,37509u,41605u,45701u,49797u,53893u,57989u,9028u,478183942u,478188038u,868u,33477u,
  37573u,41669u,45765u,49861u,53957u,58053u,478184070u,478188166u,33668u,13219u,5012u,13204u,9139u,9124u,5059u,4487u,
  24967u,21428u,33541u,37637u,41733u,45829u,49925u,54021u,58117u,478184134u,478188230u,17348u,33669u,37765u,41861u,45957u,
  50053u,54149u,58245u,33605u,37701u,41797u,45893u,49989u,54085u,58181u,33701u,37797u,41893u,45989u,50085u,54181u,
  58277u,33637u,37733u,41829u,45925u,50021u,54117u,58213u,478184198u,478188294u,276857734u,276861830u,213951366u,213955462u,33733u,37829u,
  41925u,46021u,50117u,54213u,58309u,276857670u,276861766u,213951302u,213955398u,33749u,37845u,41941u,46037u,50133u,54229u,58325u,
  276857766u,276861862u,213951398u,213955494u,33765u,37861u,41957u,46053u,50149u,54245u,58341u,276857702u,276861798u,213951334u,213955430u,142640070u,
  142644166u,144745414u,144749510u,146850758u,146854854u,81847238u,81851334u,33781u,37877u,41973u,46069u,50165u,54261u,58357u,142640086u,142644182u,
  144745430u,144749526u,146850774u,146854870u,81847254u,81851350u,142640102u,142644198u,144745446u,144749542u,146850790u,146854886u,81847270u,81851366u,142640118u,142644214u,
  144745462u,144749558u,146850806u,146854902u,81847286u,81851382u,16u,80u,208u,272u,400u,464u,592u,656u,720u,848u,
  912u,1040u,1104u,1232u,1296u,1424u,1488u,1552u,1680u,1744u,1872u,1936u,57345u,61441u,65537u,69633u,
  73729u,77825u,57361u,61457u,65553u,69649u,73745u,77841u,57377u,61473u,65569u,69665u,73761u,77857u,57393u,61489u,
  65585u,69681u,73777u,77873u,57416u,61512u,65608u,69704u,73800u,77896u,57448u,61544u,65640u,69736u,73832u,77928u,
  57474u,61570u,65666u,69762u,73858u,77954u,57538u,61634u,65730u,69826u,73922u,78018u,57602u,61698u,65794u,69890u,
  73986u,78082u,57666u,61762u,65858u,69954u,74050u,78146u,57730u,61826u,65922u,70018u,74114u,78210u,57794u,61890u,
  65986u,70082u,74178u,78274u,116u,57858u,61954u,66050u,70146u,74242u,78338u,148u,4260u,57922u,62018u,66114u,
  70210u,74306u,78402u,8388u,228u,61445u,65541u,69637u,73733u,77829u,81925u,86021u,57986u,62082u,66178u,70274u,
  74370u,78466u,4372u,308u,58050u,62146u,66242u,70338u,74434u,78530u,485531654u,485535750u,4436u,4499u,61573u,65669u,
  69765u,73861u,77957u,82053u,86149u,58114u,62210u,66306u,70402u,74498u,78594u,4500u,436u,58178u,62274u,66370u,
  70466u,74562u,78658u,485531782u,485535878u,4564u,8676u,61701u,65797u,69893u,73989u,78085u,82181u,86277u,58242u,62338u,
  66434u,70530u,74626u,78722u,7u,24583u,8740u,58306u,62402u,66498u,70594u,74690u,78786u,485531910u,485536006u,596u,
  8804u,61829u,65925u,70021u,74117u,78213u,82309u,86405u,660u,16519u,4772u,708u,485532038u,485536134u,740u,61957u,
  66053u,70149u,74245u,78341u,82437u,86533u,772u,4884u,28935u,4947u,62085u,66181u,70277u,74373u,78469u,82565u,
  86661u,836u,485532166u,485536262u,9091u,9060u,62149u,66245u,70341u,74437u,78533u,82629u,86725u,485532294u,485536390u,9076u,
  33684u,932u,17316u,13251u,12679u,13236u,29620u,62213u,66309u,70405u,74501u,78597u,82693u,86789u,485532358u,485536454u,
  25540u,62341u,66437u,70533u,74629u,78725u,82821u,86917u,62277u,66373u,70469u,74565u,78661u,82757u,86853u,62373u,
  66469u,70565u,74661u,78757u,82853u,86949u,62309u,66405u,70501u,74597u,78693u,82789u,86885u,485532422u,485536518u,284214150u,
  284218246u,221307782u,221311878u,62405u,66501u,70597u,74693u,78789u,82885u,86981u,284214086u,284218182u,221307718u,221311814u,62421u,66517u,
  70613u,74709u,78805u,82901u,86997u,284214182u,284218278u,221307814u,221311910u,62437u,66533u,70629u,74725u,78821u,82917u,87013u,
  284214118u,284218214u,221307750u,221311846u,150012870u,150016966u,152118214u,152122310u,154223558u,154227654u,89220038u,89224134u,62453u,66549u,70645u,74741u,
  78837u,82933u,87029u,150012886u,150016982u,152118230u,152122326u,154223574u,154227670u,89220054u,89224150u,150012902u,150016998u,152118246u,152122342u,154223590u,
  154227686u,89220070u,89224166u,150012918u,150017014u,152118262u,152122358u,154223606u,154227702u,89220086u,89224182u,32u,96u,224u,288u,416u,
  480u,608u,672u,736u,864u,928u,1056u,1120u,1248u,1312u,1440u,1504u,1568u,1696u,1760u,1888u,
  1952u,81921u,86017u,90113u,94209u,98305u,102401u,81937u,86033u,90129u,94225u,98321u,102417u,81953u,86049u,90145u,
  94241u,98337u,102433u,81969u,86065u,90161u,94257u,98353u,102449u,81992u,86088u,90184u,94280u,98376u,102472u,82024u,
  86120u,90216u,94312u,98408u,102504u,82050u,86146u,90242u,94338u,98434u,102530u,82114u,86210u,90306u,94402u,98498u,
  102594u,82178u,86274u,90370u,94466u,98562u,102658u,82242u,86338u,90434u,94530u,98626u,102722u,82306u,86402u,90498u,
  94594u,98690u,102786u,82370u,86466u,90562u,94658u,98754u,102850u,4212u,82434u,86530u,90626u,94722u,98818u,102914u,
  4244u,8356u,82498u,86594u,90690u,94786u,98882u,102978u,212u,4324u,90117u,94213u,98309u,102405u,106501u,110597u,
  114693u,82562u,86658u,90754u,94850u,98946u,103042u,8468u,4404u,82626u,86722u,90818u,94914u,99010u,103106u,492879878u,
  492883974u,8532u,90245u,94341u,98437u,102533u,106629u,110725u,114821u,82690u,86786u,90882u,94978u,99074u,103170u,4531u,
  8596u,4532u,82754u,86850u,90946u,95042u,99138u,103234u,492880006u,492884102u,8660u,90373u,94469u,98565u,102661u,106757u,
  110853u,114949u,82818u,86914u,91010u,95106u,99202u,103298u,532u,4103u,28679u,564u,82882u,86978u,91074u,95170u,
  99266u,103362u,492880134u,492884230u,4692u,90501u,94597u,98693u,102789u,106885u,110981u,115077u,4771u,4740u,4231u,20615u,
  8868u,4804u,492880262u,492884358u,4836u,90629u,94725u,98821u,102917u,107013u,111109u,115205u,4868u,8980u,804u,90757u,
  94853u,98949u,103045u,107141u,111237u,115333u,4932u,492880390u,492884486u,13187u,915u,9107u,13203u,90821u,94917u,99013u,
  103109u,107205u,111301u,115397u,492880518u,492884614u,4980u,17300u,13235u,13220u,9155u,8583u,29063u,25524u,90885u,94981u,
  99077u,103173u,107269u,111365u,115461u,492880582u,492884678u,21444u,91013u,95109u,99205u,103301u,107397u,111493u,115589u,90949u,
  95045u,99141u,103237u,107333u,111429u,115525u,91045u,95141u,99237u,103333u,107429u,111525u,115621u,90981u,95077u,99173u,
  103269u,107365u,111461u,115557u,492880646u,492884742u,291570566u,291574662u,228664198u,228668294u,91077u,95173u,99269u,103365u,107461u,111557u,
  115653u,291570502u,291574598u,228664134u,228668230u,91093u,95189u,99285u,103381u,107477u,111573u,115669u,291570598u,291574694u,228664230u,228668326u,
  91109u,95205u,99301u,103397u,107493u,111589u,115685u,291570534u,291574630u,228664166u,228668262u,157385670u,157389766u,159491014u,159495110u,161596358u,
  161600454u,96592838u,96596934u,91125u,95221u,99317u,103413u,107509u,111605u,115701u,157385686u,157389782u,159491030u,159495126u,161596374u,161600470u,
  96592854u,96596950u,157385702u,157389798u,159491046u,159495142u,161596390u,161600486u,96592870u,96596966u,157385718u,157389814u,159491062u,159495158u,161596406u,161600502u,
  96592886u,96596982u,48u,112u,240u,304u,432u,496u,624u,688u,752u,880u,944u,1072u,1136u,1264u,
  1328u,1456u,1520u,1584u,1712u,1776u,1904u,1968u,106497u,110593u,114689u,118785u,122881u,126977u,106513u,110609u,
  114705u,118801u,122897u,126993u,106529u,110625u,114721u,118817u,122913u,127009u,106545u,110641u,114737u,118833u,122929u,127025u,
  106568u,110664u,114760u,118856u,122952u,127048u,106600u,110696u,114792u,118888u,122984u,127080u,106626u,110722u,114818u,118914u,
  123010u,127106u,106690u,110786u,114882u,118978u,123074u,127170u,106754u,110850u,114946u,119042u,123138u,127234u,106818u,110914u,
  115010u,119106u,123202u,127298u,106882u,110978u,115074u,119170u,123266u,127362u,106946u,111042u,115138u,119234u,123330u,127426u,
  8308u,107010u,111106u,115202u,119298u,123394u,127490u,8340u,180u,107074u,111170u,115266u,119362u,123458u,127554u,4308u,
  8420u,118789u,122885u,126981u,131077u,135173u,139269u,143365u,107138u,111234u,115330u,119426u,123522u,127618u,292u,8500u,
  107202u,111298u,115394u,119490u,123586u,127682u,500228102u,500232198u,387u,4452u,118917u,123013u,127109u,131205u,135301u,139397u,
  143493u,107266u,111362u,115458u,119554u,123650u,127746u,4547u,420u,8628u,107330u,111426u,115522u,119618u,123714u,127810u,
  500228230u,500232326u,4580u,119045u,123141u,127237u,131333u,135429u,139525u,143621u,107394u,111490u,115586u,119682u,123778u,127874u,
  8724u,20487u,4644u,107458u,111554u,115650u,119746u,123842u,127938u,4723u,500228358u,500232454u,8788u,119173u,123269u,127365u,
  131461u,135557u,139653u,143749u,644u,8852u,676u,4788u,500228486u,500232582u,724u,787u,119301u,123397u,127493u,131589u,
  135685u,139781u,143877u,8964u,4359u,24839u,8996u,119429u,123525u,127621u,131717u,135813u,139909u,144005u,883u,500228614u,
  500232710u,4995u,4964u,119493u,123589u,127685u,131781u,135877u,139973u,144069u,500228742u,500232838u,884u,9108u,947u,5028u,
  21412u,391u,16775u,17332u,33716u,119557u,123653u,127749u,131845u,135941u,140037u,144133u,500228806u,500232902u,119685u,123781u,
  127877u,131973u,136069u,140165u,144261u,119621u,123717u,127813u,131909u,136005u,140101u,144197u,119717u,123813u,127909u,132005u,
  136101u,140197u,144293u,119653u,123749u,127845u,131941u,136037u,140133u,144229u,500228870u,500232966u,298926982u,298931078u,236020614u,236024710u,
  119749u,123845u,127941u,132037u,136133u,140229u,144325u,298926918u,298931014u,236020550u,236024646u,119765u,123861u,127957u,132053u,136149u,
  140245u,144341u,298927014u,298931110u,236020646u,236024742u,119781u,123877u,127973u,132069u,136165u,140261u,144357u,298926950u,298931046u,236020582u,
  236024678u,164758470u,164762566u,166863814u,166867910u,168969158u,168973254u,103965638u,103969734u,119797u,123893u,127989u,132085u,136181u,140277u,144373u,
  164758486u,164762582u,166863830u,166867926u,168969174u,168973270u,103965654u,103969750u,164758502u,164762598u,166863846u,166867942u,168969190u,168973286u,103965670u,103969766u,
  164758518u,164762614u,166863862u,166867958u,168969206u,168973302u,103965686u,103969782u,
};
__device__ __forceinline__ void bulk(const Args& a, unsigned char* lds) {
  LAS unsigned* slot = (LAS unsigned*)((LAS unsigned char*)lds + 160 * 1024 - 16);
  const int lab = blockIdx.x & 7, q0 = QOFF[lab], q1 = QOFF[lab + 1];
#pragma unroll 1
  for (;;) {
    int z_ = 0; asm volatile("" : "+v"(z_));
    unsigned char* ws = a.ws + __builtin_amdgcn_readfirstlane(z_);
    const rsrc_t rw = __builtin_amdgcn_make_buffer_rsrc(ws, 0, (int)O_DBG, 0x00020000);
    if (tid_opaque() == 0) *slot = __hip_atomic_fetch_add(ctl(ws, C_QH + lab), 1u, RLX_AGENT);
    LDSBAR();
    const int q = __builtin_amdgcn_readfirstlane(*slot) + q0;
    LDSBAR();
    if (q >= q1) break;
#define TLR()
    const unsigned e = __builtin_amdgcn_readfirstlane(QT[q]);
    const int ty = e & 15, p1 = (e >> 4) & 255, p2 = (e >> 12) & 255, p3 = (e >> 20) & 63, p4 = e >> 26;
    if (ty == T_LN1) { t_ln<false, 1>(a, ws, rw, lds, p1); dep_post(ws, C_LN1 + (p1 >> 3), 1); }
    else if (ty == T_AS) { dep_wait(ws, C_LN1 + (p1 >> 2), 8); TLR(); t_a<1>(a, ws, rw, lds, p1, p2); dep_post(ws, C_A + p1, 1); }
    else if (ty == T_AM) { dep_wait(ws, C_LN1 + (p1 >> 2), 8); TLR(); t_a<2>(a, ws, rw, lds, p1, p2); dep_post(ws, C_A + p1, 2); }
    else if (ty == T_AB) { dep_wait(ws, C_LN1 + (p1 >> 2), 8); TLR(); t_a<4>(a, ws, rw, lds, p1, p2); dep_post(ws, C_A + p1, 4); }
    else if (ty == T_B1A) { dep_wait(ws, C_PROG + p1, 64); TLR(); t_b1a(a, ws, rw, p1, p2, p1 < B1F ? 2 : 1); dep_post(ws, C_B1A + p1, 1); }
    else if (ty == T_B1B) {
      dep_wait(ws, C_B1A + p1, p1 < B1F ? 2 : 4); TLR();
      const int pol = (int)__builtin_amdgcn_readfirstlane(__hip_atomic_load(ctl(ws, C_POL), RLX_AGENT)) - 1;
      if (p1 < B1F) t_b1b<3>(a, ws, rw, lds, p1, p2, pol); else t_b1b<1>(a, ws, rw, lds, p1, p2, pol);
      waitv<0>(); LDSBAR();
      if (tid_opaque() == 0) *slot = __hip_atomic_fetch_add(ctl(ws, C_B1B + p1), 1u, RLX_AGENT);
      LDSBAR();
      const unsigned arr = __builtin_amdgcn_readfirstlane(*slot);
      if (arr == (p1 < B1F ? 2u : 8u)) {
        if (__builtin_amdgcn_readfirstlane(tid_opaque()) < 64) { __builtin_amdgcn_fence(__ATOMIC_ACQUIRE, "agent"); waitv<0>(); }
        LDSBAR();
        t_ln<true, 2>(a, ws, rw, lds, p1 * 2);
        dep_post(ws, C_LN2 + unit_of(p1), 1);
      } else LDSBAR();
    } else {
      const int s0 = p1, u = unit_of(s0), cls = s0 < SEG0 ? 0 : s0 < SEG1 ? 1 : s0 < SEG2 ? 2 : 3;
      const int nseg = 8 >> cls, nsl = cls < 2 ? 8 : cls == 2 ? 12 : 20;
      const int srow = cls < 2 ? 256 * s0 : cls == 2 ? 256 * SEG1 + 384 * (s0 - SEG1) : 256 * SEG1 + 384 * (SEG2 - SEG1) + 640 * (s0 - SEG2);
      if (ty == T_G1) {
        dep_wait(ws, C_LN2 + u, nseg); TLR();
        if (cls == 0) t_g1x(a, ws, rw, lds, s0, p2); else if (cls == 1) t_g1<4>(a, ws, rw, lds, s0, p2); else if (cls == 2) t_g1<2>(a, ws, rw, lds, s0, p2); else t_g1<1>(a, ws, rw, lds, s0, p2);
        dep_post(ws, C_G1 + u, 1);
      } else if (ty == T_G2) {
        dep_wait(ws, C_G1 + u, 36); TLR();
        const int sl = p2 >> 1, k0 = p3 * 4, nk = p4 * 4, sr = srow + sl * nseg * 32;
        if (p2 & 1) { if (cls == 0) t_g2x<4>(a, ws, rw, lds, s0, k0, nk, sr); else if (cls == 1) t_g2<4, 4>(a, ws, rw, lds, s0, k0, nk, sr); else if (cls == 2) t_g2<2, 4>(a, ws, rw, lds, s0, k0, nk, sr); else t_g2<1, 4>(a, ws, rw, lds, s0, k0, nk, sr); }
        else { if (cls == 0) t_g2x<5>(a, ws, rw, lds, s0, k0, nk, sr); else if (cls == 1) t_g2<4, 5>(a, ws, rw, lds, s0, k0, nk, sr); else if (cls == 2) t_g2<2, 5>(a, ws, rw, lds, s0, k0, nk, sr); else t_g2<1, 5>(a, ws, rw, lds, s0, k0, nk, sr); }
        dep_post(ws, C_G2 + u, 1);
      } else {
        dep_wait(ws, C_G2 + u, 2 * nsl); TLR();
        if (cls == 0) t_fin<8>(a, ws, s0 * 32 + p2 * 32, srow + p2 * 32, 256, 9);
        else if (cls == 1) t_fin<8>(a, ws, s0 * 32 + p2 * 8, srow + p2 * 8, 128, 3);
        else if (cls == 2) t_fin<12>(a, ws, s0 * 32 + p2 * 8, srow + p2 * 8, 64, 3);
        else t_fin<20>(a, ws, s0 * 32 + p2 * 8, srow + p2 * 8, 32, 3);
      }
    }
  }
}

constexpr int L_MEM = 0;
constexpr int L_TQ = 9216;
constexpr int L_TK = 11264;
constexpr int L_CP = 13312;
constexpr int L_DUMP = 15360;
constexpr int L_KX = 16384;
constexpr int L_T0 = 32768;
constexpr int REDW = 2;

template <int ROLE, int WR>
__device__ __forceinline__ void chain_loop(const int role, const int wave, const int lane, const v8u (&w1)[9], const v8u (&w2)[10],
                                           const Args& a, unsigned char* lds, const int pol) {
  unsigned char* ws = a.ws;
  const int g = lane >> 4, c16 = lane & 15;
  const int h = role & 31, cj = role % 36, rh = role / 36;
  LAS unsigned char* L = (LAS unsigned char*)lds;
  unsigned char* xb = xch_base(ws);
  const rsrc_t rw = __builtin_amdgcn_make_buffer_rsrc(ws, 0, (int)O_DBG, 0x00020000);
  const __amdgpu_buffer_rsrc_t rsM = __builtin_amdgcn_make_buffer_rsrc(xb + X_MEM, 0, NSEG * MEM_SLOT, 0x00020000);
  const __amdgpu_buffer_rsrc_t rsP = __builtin_amdgcn_make_buffer_rsrc(xb + X_P, 0, NSEG * P_SLOT, 0x00020000);
  const __amdgpu_buffer_rsrc_t rsV = __builtin_amdgcn_make_buffer_rsrc(xb + X_VM, 0, 22 * 2097152, 0x00020000);
  GAS unsigned* tmo = ctl(ws, C_TMO);
  const v4u PSN = {SENT, SENT, SENT, SENT};
  const int idx = (lane & 8) ? 0xEEEE : 0x4444;
  auto mem_off = [](int l) { const int ga = l >> 4, hf = (l >> 3) & 1, i = l & 7; return (ga * 2 + (i >> 2)) * 128 + hf * 64 + (i & 3) * 16; };
  const unsigned moff0 = lmap(wave * 1024 + mem_off(lane)), moff1 = lane < 8 ? lmap(8192 + mem_off(wave * 8 + lane)) : OOB;
  const int lm1 = lane < 8 ? L_MEM + 8192 + wave * 128 + lane * 16 : L_DUMP + lane * 16;
  const unsigned phead = (4 * wave + g) * 1024 + (4 * rh + (lane & 3)) * 16;
  const unsigned pqoff = lmap(phead + (c16 >> 2) * 128), pmoff = c16 < 4 ? lmap(phead + 512) : OOB;
  const unsigned vmoff = cj * 16384 + wave * 1024 + lane * 16;
  const unsigned mpub = (lane < 48 && (lane & 8) == 0) ? lmap((cj * 2 + rh) * 128 + (lane & 7) * 16) + (lane >> 4) * MEM_REP : OOB;
  const int cpoff = (lane & 3) * 64 + ((lane >> 2) & 1) * 32 + (lane >> 3) * 4;
  const int cpw = c16 < 4 ? L_CP + wave * 256 + c16 * 64 + g * 16 : L_DUMP + lane * 16;
  const unsigned voxo = (unsigned)O_VF + (cj * 32 + 4 * wave) * 1024 + lane * 16;
  const int troff = (4 * g + (c16 >> 2)) * 16 + (lane & 1) * 8;
  const float SCL = 0.08838834764831845f * 1.4426950408889634f;
#define STAMP(k)
#define MF(c9) (*(LAS v4u*)(L + L_MEM + (c9) * 1024 + lane * 16))
  if (ROLE == 0 && WR == 1) {
#pragma unroll
    for (int i = 0; i < 8; ++i) *(LAS v4u*)(L + L_KX + i * 1024 + lane * 16) = ld16(rw, (unsigned)O_K + h * 8192 + i * 1024 + lane * 16, 0);
  }
  if (ROLE == 0 && WR < 3) __builtin_amdgcn_s_setprio(2);
  unsigned T1 = now32() - (unsigned)a.x1;
  for (int s = 0; s < NSEG; ++s) {
    STAMP(0);
    const int sn = s < NSEG - 1 ? s : NSEG - 2;
    const unsigned vp = (unsigned)sn * (36 * 32 * 1024);
    v4u vox[4];
    {
      const unsigned so = s * MEM_SLOT + half_off(xb + X_MEM + (size_t)s * MEM_SLOT, pol);
      const unsigned so1 = so + MEM_REP, so2 = so + 2 * MEM_REP;
      wait_until(T1 + a.x1);
      v4u m0 = __builtin_amdgcn_raw_buffer_load_b128(rsM, moff0, so, 16), m1 = __builtin_amdgcn_raw_buffer_load_b128(rsM, moff1, so, 16);
      asm volatile("" ::: "memory");
      if (ROLE != 2) { *(LAS v4u*)(L + L_MEM + wave * 1024 + lane * 16) = m0; *(LAS v4u*)(L + lm1) = m1; }
      int sp = 0;
      if (__builtin_expect(__any(max(vmax4(m0), vmax4(m1)) == SENT), 0)) {
        do {
          if (spin_fail(sp, tmo, 1u)) break;
          if (sp > 2) __builtin_amdgcn_s_sleep(2);
          const unsigned sx = sp == 1 ? so1 : so2;
          m0 = __builtin_amdgcn_raw_buffer_load_b128(rsM, moff0, sx, 16); m1 = __builtin_amdgcn_raw_buffer_load_b128(rsM, moff1, sx, 16);
          asm volatile("" ::: "memory");
        } while (__any(max(vmax4(m0), vmax4(m1)) == SENT));
        if (ROLE != 2) { *(LAS v4u*)(L + L_MEM + wave * 1024 + lane * 16) = m0; *(LAS v4u*)(L + lm1) = m1; }
      }
      LDSBAR();
    }
    unsigned T0 = 0;
    if (ROLE == 2) T0 = now32();
    else if (wave == 7) *(LAS unsigned*)(L + L_T0) = now32();
    STAMP(1);
    const unsigned sp_ = s * P_SLOT + half_off(xb + X_P + (size_t)s * P_SLOT, pol);
    const unsigned sv_ = vm_slot(s) + half_off(xb + X_VM + vm_slot(s), pol);
#define GLDT(dst, ptr, imm) dst = ld16(rw, voxo + (imm), ptr)
#define VOX4() do { GLDT(vox[0], vp, 0); GLDT(vox[1], vp, 1024); GLDT(vox[2], vp, 2048); GLDT(vox[3], vp, 3072); SB(); } while (0)
    unsigned ca = 32u;
#define HK() do { \
      if (wave == REDW && s + 2 < NSEG) __builtin_amdgcn_raw_buffer_store_b128(PSN, rsM, mpub, (s + 2) * MEM_SLOT + half_off(xb + X_MEM + (size_t)(s + 2) * MEM_SLOT, pol), 16); \
      if (wave == 3) { \
        if (ROLE == 0 && s + 2 < NSEG) { \
          const unsigned s2 = (s + 2) * P_SLOT + half_off(xb + X_P + (size_t)(s + 2) * P_SLOT, pol); \
          _Pragma("unroll") for (int r = 0; r < 3; ++r) __builtin_amdgcn_raw_buffer_store_b128(PSN, rsP, lane < 40 ? lmap(h * 1024 + lane * 16) + r * P_REP : OOB, s2, 16); \
        } \
        if (ROLE == 2 && role == 65 && s > 0 && lane == 0) __hip_atomic_store(ctl(ws, C_PROG + s - 1), 64u, RLX_AGENT); \
        if (s + 2 < NSEG) ca = __hip_atomic_load(ctl(ws, C_A + s + 2), RLX_AGENT); \
      } \
      SB(); } while (0)
    if (ROLE == 0) {
      const int toff = ((wave & 3) * 32 + (lane >> 5) * 16 + c16) * 16 + (g & 1) * 8;
      f4 a0 = {0, 0, 0, 0}, a1 = {0, 0, 0, 0};
      v4u mf[9];
#pragma unroll
      for (int c9 = 0; c9 < 9; ++c9) mf[c9] = MF(c9);
      SB();
      if (WR < 3) {
        v4u kx[8];
#pragma unroll
        for (int c9 = 0; c9 < 9; ++c9) {
          a0 = smf(mf[c9], w1[c9], a0, idx); a1 = smf(mf[c9], w2[c9], a1, idx);
          if (WR == 0) {
            if (c9 == 0) GLDT(vox[0], vp, 0);
            if (c9 == 1) GLDT(vox[1], vp, 1024);
            if (c9 == 2) GLDT(vox[2], vp, 2048);
            if (c9 == 3) GLDT(vox[3], vp, 3072);
            if (c9 >= 5) { kx[2 * (c9 - 5)] = *(LAS v4u*)(L + L_KX + (s & 1) * 8192 + (2 * (c9 - 5)) * 1024 + lane * 16); kx[2 * (c9 - 5) + 1] = *(LAS v4u*)(L + L_KX + (s & 1) * 8192 + (2 * (c9 - 5) + 1) * 1024 + lane * 16); }
            SB();
          }
        }
        { f4 t; _Pragma("unroll") for (int r = 0; r < 4; ++r) t[r] = sw32add(a0[r], a1[r]); *(LAS v2u*)(L + L_TQ + toff) = pack4(t); }
        LDSBAR();
        STAMP(5);
        if (WR == 0) {
          v4u qf[4];
#pragma unroll
          for (int wp = 0; wp < 4; ++wp) qf[wp] = cat(trread(L + L_TQ + troff + wp * 512), trread(L + L_TQ + troff + wp * 512 + 256));
          f4 s0 = {0, 0, 0, 0}, s1 = {0, 0, 0, 0};
#pragma unroll
          for (int wp = 0; wp < 4; ++wp) { s0 = mfma32(kx[2 * wp], qf[wp], s0); s1 = mfma32(kx[2 * wp + 1], qf[wp], s1); }
          s0 *= SCL; s1 *= SCL;
          float mxx = fmaxf(fmaxf(fmaxf(s0[0], s0[1]), fmaxf(s0[2], s0[3])), fmaxf(fmaxf(s1[0], s1[1]), fmaxf(s1[2], s1[3])));
          mxx = g_max(mxx);
          asm volatile("" : "+v"(s0), "+v"(s1));
          STAMP(6);
          RAWBAR();
          STAMP(7);
          v4u kf[4];
#pragma unroll
          for (int wp = 0; wp < 4; ++wp) kf[wp] = cat(trread(L + L_TK + troff + wp * 512), trread(L + L_TK + troff + wp * 512 + 256));
          SB();
          float e[8];
#pragma unroll
          for (int r = 0; r < 4; ++r) { e[r] = __builtin_amdgcn_exp2f(s0[r] - mxx); e[4 + r] = __builtin_amdgcn_exp2f(s1[r] - mxx); }
          float zx = ((e[0] + e[1]) + (e[2] + e[3])) + ((e[4] + e[5]) + (e[6] + e[7]));
          asm volatile("" : "+v"(zx));
          SB();
          f4 sm = {0, 0, 0, 0};
#pragma unroll
          for (int wp = 0; wp < 4; ++wp) sm = mfma32(kf[wp], qf[wp], sm);
          float em[4];
#pragma unroll
          for (int r = 0; r < 4; ++r) em[r] = __builtin_amdgcn_exp2f(__builtin_fmaf(sm[r], SCL, -mxx));
          float zt = g_sum(__builtin_fmaf(0.5f, (em[0] + em[1]) + (em[2] + em[3]), zx));
          float rz = __builtin_amdgcn_rcpf(zt);
          v4u o; v2u om;
          o.x = pack2(e[0] * rz, e[1] * rz); o.y = pack2(e[2] * rz, e[3] * rz); o.z = pack2(e[4] * rz, e[5] * rz); o.w = pack2(e[6] * rz, e[7] * rz);
          om.x = pack2(em[0] * rz, em[1] * rz); om.y = pack2(em[2] * rz, em[3] * rz);
          if (__builtin_expect(__any(!(zt < 1e30f)), 0)) {
            float mt = fmaxf(fmaxf(fmaxf(sm[0], sm[1]), fmaxf(sm[2], sm[3])) * SCL, mxx);
            mt = g_max(mt);
#pragma unroll
            for (int r = 0; r < 4; ++r) { e[r] = __builtin_amdgcn_exp2f(s0[r] - mt); e[4 + r] = __builtin_amdgcn_exp2f(s1[r] - mt); em[r] = __builtin_amdgcn_exp2f(__builtin_fmaf(sm[r], SCL, -mt)); }
            zt = g_sum(__builtin_fmaf(0.5f, (em[0] + em[1]) + (em[2] + em[3]), ((e[0] + e[1]) + (e[2] + e[3])) + ((e[4] + e[5]) + (e[6] + e[7]))));
            rz = __builtin_amdgcn_rcpf(zt);
            o.x = fixs(pack2(e[0] * rz, e[1] * rz)); o.y = fixs(pack2(e[2] * rz, e[3] * rz)); o.z = fixs(pack2(e[4] * rz, e[5] * rz)); o.w = fixs(pack2(e[6] * rz, e[7] * rz));
            om.x = fixs(pack2(em[0] * rz, em[1] * rz)); om.y = fixs(pack2(em[2] * rz, em[3] * rz));
          }
          const unsigned pa = lmap(h * 1024 + (c16 & 7) * 16);
          __builtin_amdgcn_raw_buffer_store_b128(o, rsP, pa + (c16 >> 3) * P_REP + g * 128, sp_, 16);
          const auto mx_ = __builtin_amdgcn_permlane16_swap(om.x, om.x, false, false);
          const auto my_ = __builtin_amdgcn_permlane16_swap(om.y, om.y, false, false);
          v4u o2; o2.x = mx_[0]; o2.y = my_[0]; o2.z = mx_[1]; o2.w = my_[1];
          __builtin_amdgcn_raw_buffer_store_b128(o2, rsP, (lane < 24) ? pa + 512 + (lane >> 3) * P_REP : OOB, sp_, 16);
          __builtin_amdgcn_raw_buffer_store_b128(o, rsP, c16 < 8 ? pa + 2 * P_REP + g * 128 : OOB, sp_, 16);
          SB();
        } else {
          VOX4();
          v4u kn[8];
          if (WR == 1) {
#pragma unroll
            for (int i = 0; i < 8; ++i) kn[i] = ld16(rw, (unsigned)O_K + h * 8192 + i * 1024 + lane * 16, (unsigned)(sn + 1) * (32 * 8192));
          }
          if (WR == 2) HK();
          RAWBAR();
          if (WR == 1) {
#pragma unroll
            for (int wp = 0; wp < 4; ++wp) {
              const v4u kf = cat(trread(L + L_TK + troff + wp * 512), trread(L + L_TK + troff + wp * 512 + 256));
              st16(rw, c16 < 8 ? (unsigned)O_KM + (((s * 32 + h) * 4 + wp) * 4 + g) * 128 + c16 * 16 : OOB, kf);
            }
#pragma unroll
            for (int i = 0; i < 8; ++i) *(LAS v4u*)(L + L_KX + ((s + 1) & 1) * 8192 + i * 1024 + lane * 16) = kn[i];
          }
        }
      } else {
        VOX4();
#pragma unroll
        for (int c9 = 0; c9 < 2; ++c9) { a0 = smf(mf[c9], w1[c9], a0, idx); a1 = smf(mf[c9], w2[c9], a1, idx); }
        asm volatile("" : "+v"(a0), "+v"(a1));
        RAWBAR();
#pragma unroll
        for (int c9 = 2; c9 < 9; ++c9) { a0 = smf(mf[c9], w1[c9], a0, idx); a1 = smf(mf[c9], w2[c9], a1, idx); }
        { f4 t; _Pragma("unroll") for (int r = 0; r < 4; ++r) t[r] = sw32add(a0[r], a1[r]); *(LAS v2u*)(L + L_TK + toff) = pack4(t); }
        LDSBAR();
      }
    } else if (ROLE == 1) {
      f4 a0 = {0, 0, 0, 0};
      v4u mf[9];
#pragma unroll
      for (int c9 = 0; c9 < 9; ++c9) mf[c9] = MF(c9);
      GLDT(vox[0], vp, 0);
      if (s + 2 < NSEG) {
        const unsigned s2 = vm_slot(s + 2) + half_off(xb + X_VM + vm_slot(s + 2), pol);
        __builtin_amdgcn_raw_buffer_store_b128(PSN, rsV, (wave + 8 * g) * 16384 + h * 256 + c16 * 16, s2, 16);
        if (WR == 0) __builtin_amdgcn_raw_buffer_store_b128(PSN, rsV, g == 0 ? (wave + 32) * 16384 + h * 256 + c16 * 16 : OOB, s2, 16);
      }
      if (WR == 0) HK();
      SB();
#pragma unroll
      for (int c9 = 0; c9 < 9; ++c9) {
        a0 = smf(mf[c9], w1[c9], a0, idx);
        if (c9 == 3) { GLDT(vox[1], vp, 1024); SB(); }
        if (c9 == 7) { GLDT(vox[2], vp, 2048); SB(); }
      }
      *(LAS unsigned*)(L + L_TQ + (wave * 16 + c16) * 16 + (g & 1) * 8 + (g >> 1) * 4) = pack2(sw32add(a0[0], a0[2]), sw32add(a0[1], a0[3]));
      GLDT(vox[3], vp, 3072);
      LDSBAR();
      STAMP(5);
      const int tv = (16 * g + 8 * ((lane >> 1) & 1) + (c16 >> 2)) * 16 + (lane & 1) * 8;
      v4u vf[2];
#pragma unroll
      for (int b = 0; b < 2; ++b) vf[b] = cat(trread(L + L_TQ + tv + b * 1024), trread(L + L_TQ + tv + b * 1024 + 64));
      f4 t[5];
#pragma unroll
      for (int n = 0; n < 5; ++n) t[n] = f4{0, 0, 0, 0};
#pragma unroll
      for (int b = 0; b < 2; ++b)
#pragma unroll
        for (int n = 0; n < 4; ++n) t[n] = smf(vf[b], w2[n * 2 + b], t[n], idx);
      if (WR == 0) { t[4] = smf(vf[0], w2[8], t[4], idx); t[4] = smf(vf[1], w2[9], t[4], idx); }
      {
        f4 x, y;
#pragma unroll
        for (int r = 0; r < 4; ++r) { x[r] = sw32add(t[0][r], t[1][r]); y[r] = sw32add(t[2][r], t[3][r]); }
        const v2u xp = pack4(x), yp = pack4(y);
        const auto sx = __builtin_amdgcn_permlane16_swap(fixs(xp.x), fixs(yp.x), false, false);
        const auto sy = __builtin_amdgcn_permlane16_swap(fixs(xp.y), fixs(yp.y), false, false);
        v4u o; o.x = sx[0]; o.y = sy[0]; o.z = sx[1]; o.w = sy[1];
        const int n = ((g & 1) << 1) | (g >> 1);
        __builtin_amdgcn_raw_buffer_store_b128(o, rsV, (wave + 8 * n) * 16384 + h * 256 + c16 * 16, sv_, 16);
      }
      if (WR == 0) {
        f4 x;
#pragma unroll
        for (int r = 0; r < 4; ++r) x[r] = sw32add(t[4][r], t[4][r]);
        const v2u xp = pack4(x);
        const auto sx = __builtin_amdgcn_permlane16_swap(fixs(xp.x), fixs(xp.x), false, false);
        const auto sy = __builtin_amdgcn_permlane16_swap(fixs(xp.y), fixs(xp.y), false, false);
        v4u o; o.x = sx[0]; o.y = sy[0]; o.z = sx[1]; o.w = sy[1];
        __builtin_amdgcn_raw_buffer_store_b128(o, rsV, g == 0 ? (wave + 32) * 16384 + h * 256 + c16 * 16 : OOB, sv_, 16);
      }
      SB();
    } else {
      VOX4();
      HK();
    }
    if (s == NSEG - 1) {
      waitv<0>(); LDSBAR();
      if (ROLE != 2 && wave == 3 && lane == 0) __hip_atomic_fetch_add(ctl(ws, C_PROG + s), 1u, RLX_AGENT);
      break;
    }
    if (wave == 3 && __builtin_expect(__builtin_amdgcn_readfirstlane(ca) < 32u, 0)) {
      int sp = 0;
      while (__builtin_amdgcn_readfirstlane(__hip_atomic_load(ctl(ws, C_A + s + 2), RLX_AGENT)) < 32u) { if (spin_fail(sp, tmo, 3u)) break; __builtin_amdgcn_s_sleep(4); }
    }
    STAMP(2);
    {
      if (ROLE != 2) T0 = __builtin_amdgcn_readfirstlane(*(LAS unsigned*)(L + L_T0));
      const unsigned sp1 = sp_ + P_REP, sp2 = sp_ + 2 * P_REP;
      v4u pq, pm, vm;
      asm volatile("" :: "v"(vox[0]), "v"(vox[1]), "v"(vox[2]), "v"(vox[3]));
      wait_until(T0 + a.x2);
      STAMP(8);
      pq = __builtin_amdgcn_raw_buffer_load_b128(rsP, pqoff, sp_, 16); pm = __builtin_amdgcn_raw_buffer_load_b128(rsP, pmoff, sp_, 16);
      vm = __builtin_amdgcn_raw_buffer_load_b128(rsV, vmoff, sv_, 16);
      asm volatile("" ::: "memory");
      const f4 z = {0, 0, 0, 0};
      f4 a0 = mfma32(vox[0], pq, z), a1 = mfma32(vox[1], pq, z), a2 = mfma32(vox[2], pq, z), a3 = mfma32(vox[3], pq, z);
      a0 = mfma32(vm, pm, a0);
      int sp = 0;
      if (__builtin_expect(__any(umax3(vmax4(pq), vmax4(pm), vmax4(vm)) == SENT), 0)) {
        do {
          if (spin_fail(sp, tmo, 2u)) break;
          if (sp > 2) __builtin_amdgcn_s_sleep(2);
          const unsigned sx = sp == 1 ? sp1 : sp2;
          pq = __builtin_amdgcn_raw_buffer_load_b128(rsP, pqoff, sx, 16); pm = __builtin_amdgcn_raw_buffer_load_b128(rsP, pmoff, sx, 16);
          vm = __builtin_amdgcn_raw_buffer_load_b128(rsV, vmoff, sv_, 16);
          asm volatile("" ::: "memory");
        } while (__any(umax3(vmax4(pq), vmax4(pm), vmax4(vm)) == SENT));
        a0 = mfma32(vox[0], pq, z); a1 = mfma32(vox[1], pq, z); a2 = mfma32(vox[2], pq, z); a3 = mfma32(vox[3], pq, z);
        a0 = mfma32(vm, pm, a0);
      }
      STAMP(3);
      f4 o;
#pragma unroll
      for (int r = 0; r < 4; ++r) o[r] = (a0[r] + dppf<0x104>(a1[r])) + (dppf<0x108>(a2[r]) + dppf<0x10C>(a3[r]));
      *(LAS f4*)(L + cpw) = o;
      LDSBAR();
      if (wave == REDW) {
        float pv[8];
#pragma unroll
        for (int w = 0; w < 8; ++w) pv[w] = *(LAS float*)(L + L_CP + w * 256 + cpoff);
        const unsigned so = (s + 1) * MEM_SLOT + half_off(xb + X_MEM + (size_t)(s + 1) * MEM_SLOT, pol);
        const float v = ((pv[0] + pv[1]) + (pv[2] + pv[3])) + ((pv[4] + pv[5]) + (pv[6] + pv[7]));
        const unsigned pr = fixs(pack2(v, dppf<0x108>(v)));
        const auto a_ = __builtin_amdgcn_permlane16_swap(pr, pr, false, false);
        const auto b_ = __builtin_amdgcn_permlane32_swap(a_[0], a_[0], false, false);
        const auto c_ = __builtin_amdgcn_permlane32_swap(a_[1], a_[1], false, false);
        v4u ov; ov.x = b_[0]; ov.y = c_[0]; ov.z = b_[1]; ov.w = c_[1];
        __builtin_amdgcn_raw_buffer_store_b128(ov, rsM, mpub, so, 16);
        T1 = now32() - 230u;
      } else {
        T1 = now32();
      }
      STAMP(9);
    }
  }
}

template <int ROLE>
__device__ __forceinline__ void chain_body(const int role, const Args& a, unsigned char* lds) {
  unsigned char* ws = a.ws;
  const int tid = threadIdx.x, wave = __builtin_amdgcn_readfirstlane(tid >> 6), lane = tid & 63;
  const int g = lane >> 4, q = (lane & 15) >> 2, p = lane & 3;
  const int h = role & 31;
  LAS unsigned char* L = (LAS unsigned char*)lds;
  const unsigned ldsw = (unsigned)(size_t)L + wave * 1024;
  v8u w1[9], w2[10];
  const int r32 = 16 * (g >> 1) + 4 * (g & 1) + (q & 1) + 8 * (q >> 1);
  auto toff = [&](int row, int ct_) { return row * 128 + ((ct_ ^ ((row >> 1) & 3)) << 5) + ((p >> 1) << 4) + ((p & 1) << 3); };
  if (ROLE == 0) {
    const unsigned so = stage_src(lane, 8 * wave, HD_);
    const int blk = (wave >> 2) * 2 + ((wave & 3) >> 1), ct0 = 2 * (wave & 1);
#pragma unroll
    for (int rd = 0; rd < 2; ++rd) {
      const int k0 = rd * 5, kn = rd == 0 ? 5 : 4;
#pragma unroll
      for (int kt = 0; kt < kn; ++kt)
#pragma unroll
        for (int b = 0; b < 4; ++b) glds_s((b < 2 ? a.Wq : a.Wk) + (size_t)(k0 + kt) * 64 * HD_ + h * 128 + (b & 1) * 64, so, ldsw + kt * 32768 + b * 8192);
      waitv<0>(); LDSBAR();
#pragma unroll
      for (int kt = 0; kt < kn; ++kt) {
        v2u x[4], y[4];
#pragma unroll
        for (int u = 0; u < 4; ++u) {
          const int row = 32 * (u >> 1) + r32 + 2 * (u & 1);
          x[u] = trread(L + kt * 32768 + blk * 8192 + toff(row, ct0));
          y[u] = trread(L + kt * 32768 + blk * 8192 + toff(row, ct0 + 1));
        }
        w1[k0 + kt] = cat8(x[0], x[1], x[2], x[3]);
        w2[k0 + kt] = cat8(y[0], y[1], y[2], y[3]);
      }
      LDSBAR();
    }
  } else if (ROLE == 1) {
    const unsigned so = stage_src(lane, 8 * wave, HD_);
#pragma unroll
    for (int kt = 0; kt < 9; ++kt)
#pragma unroll
      for (int b = 0; b < 2; ++b) glds_s(a.Wv + (size_t)kt * 64 * HD_ + h * 128 + b * 64, so, ldsw + kt * 16384 + b * 8192);
    waitv<0>(); LDSBAR();
#pragma unroll
    for (int kt = 0; kt < 9; ++kt) {
      v2u x[4];
#pragma unroll
      for (int u = 0; u < 4; ++u) x[u] = trread(L + kt * 16384 + (wave >> 2) * 8192 + toff(32 * (u >> 1) + r32 + 2 * (u & 1), wave & 3));
      w1[kt] = cat8(x[0], x[1], x[2], x[3]);
    }
    LDSBAR();
    const unsigned so2 = stage_src(lane, 8 * wave, D_);
#pragma unroll
    for (int it = 0; it < 18; ++it) glds_s(a.Wo + (size_t)(h * 128 + 64 * (it & 1)) * D_ + (it >> 1) * 64, so2, ldsw + it * 8192);
    waitv<0>(); LDSBAR();
#pragma unroll
    for (int n = 0; n < 5; ++n) {
      int nt = wave + 8 * n; nt = nt < 36 ? nt : 35;
#pragma unroll
      for (int b = 0; b < 2; ++b) {
        v2u x[4];
#pragma unroll
        for (int u = 0; u < 4; ++u) x[u] = trread(L + (nt >> 2) * 16384 + b * 8192 + toff(32 * (u >> 1) + r32 + 2 * (u & 1), nt & 3));
        w2[n * 2 + b] = cat8(x[0], x[1], x[2], x[3]);
      }
    }
    LDSBAR();
  } else if (role == 64 && wave == 0) {
    unsigned char* xb = xch_base(ws);
    const rsrc_t rx = __builtin_amdgcn_make_buffer_rsrc(xb + X_MEM + 3 * MEM_REP, 0, MEM_REP, 0x00020000);
    unsigned best[2] = {~0u, ~0u};
    for (int i = 0; i < 16; ++i) {
      const unsigned off = (i & 1) * 8192 + (i >> 1) * 1024 + ((i >> 3) & 1) * 16384;
      st16(rx, lane < 8 ? off + lane * 16 : OOB, v4u{1u, 2u, 3u, 4u});
      waitv<0>();
      const unsigned t0 = now32();
      const v4u v = ld16(rx, off, 0);
      asm volatile("s_waitcnt vmcnt(0)" :: "v"(v) : "memory");
      const unsigned dt = now32() - t0;
      if (i & 1) best[1] = min(best[1], dt); else best[0] = min(best[0], dt);
    }
    const unsigned nh = best[1] < best[0] ? 1u : 0u;
    if (lane == 0) { *ctl(ws, 3) = best[0]; *ctl(ws, 4) = best[1]; __hip_atomic_store(ctl(ws, C_POL), a.k1 ? (unsigned)a.k1 : 1u + (nh ^ ((unsigned)((size_t)xb >> 21) & 1u)), RLX_AGENT); }
  }
  LAS unsigned* slot = (LAS unsigned*)(L + L_DUMP);
  if (wave == 0) {
    int sp = 0; unsigned pv;
    while ((pv = __builtin_amdgcn_readfirstlane(__hip_atomic_load(ctl(ws, C_POL), RLX_AGENT))) == 0u) { if (spin_fail(sp, ctl(ws, C_TMO), 4u)) { pv = 1u; break; } __builtin_amdgcn_s_sleep(4); }
    for (int i = 0; i < 2; ++i) while (__builtin_amdgcn_readfirstlane(__hip_atomic_load(ctl(ws, C_A + i), RLX_AGENT)) < 32u) { if (spin_fail(sp, ctl(ws, C_TMO), 5u)) break; __builtin_amdgcn_s_sleep(4); }
    if (lane == 0) *slot = pv - 1u;
  }
  LDSBAR();
  const int pol = __builtin_amdgcn_readfirstlane(*slot);
  LDSBAR();
  if (ROLE == 0) {
    if (wave == 0) chain_loop<0, 0>(role, wave, lane, w1, w2, a, lds, pol);
    else if (wave == 1) chain_loop<0, 1>(role, wave, lane, w1, w2, a, lds, pol);
    else if (wave < 4) chain_loop<0, 2>(role, wave, lane, w1, w2, a, lds, pol);
    else chain_loop<0, 3>(role, wave, lane, w1, w2, a, lds, pol);
    __builtin_amdgcn_s_setprio(0);
  } else if (ROLE == 1) {
    if (wave < 4) chain_loop<1, 0>(role, wave, lane, w1, w2, a, lds, pol);
    else chain_loop<1, 1>(role, wave, lane, w1, w2, a, lds, pol);
  } else {
    chain_loop<2, 0>(role, wave, lane, w1, w2, a, lds, pol);
  }
}

__global__ void __launch_bounds__(512, 2) k_main(Args a) {
  extern __shared__ __attribute__((aligned(16))) unsigned char lds[];
  const int b = blockIdx.x;
  const int x = b & 7, r = b >> 3;
  if (x < 4 && r < 18) {
    const int role = r * 4 + x;
    if (role < 32) chain_body<0>(role, a, lds);
    else if (role < 64) chain_body<1>(role, a, lds);
    else chain_body<2>(role, a, lds);
    LDSBAR();
  }
  else {
    const int nh = x < 4 ? 2 : 6, h0 = x < 4 ? 2 * x : 8 + 6 * (x - 4), nw = x < 4 ? 14 : 32, rk = x < 4 ? r - 18 : r;
    const int wave = __builtin_amdgcn_readfirstlane(threadIdx.x >> 6), lane = threadIdx.x & 63;
    const unsigned ldsw = (unsigned)(size_t)(LAS unsigned char*)lds + 131072 + wave * 1024;
    const int np1 = 3 * 144 * nh, np2 = nh * 144;
    for (int i = rk * 8 + wave; i < np1 + np2; i += nw * 8) {
      if (i < np1) {
        const int m = i / (144 * nh), rem = i % (144 * nh), hh = rem / 144, r4 = rem % 144;
        glds_s((m == 0 ? a.Wq : m == 1 ? a.Wk : a.Wv) + (size_t)r4 * 4 * HD_ + (h0 + hh) * 128, (lane >> 4) * (HD_ * 2) + (lane & 15) * 16, ldsw);
      } else glds_s((const unsigned char*)(a.Wo + (size_t)h0 * 128 * D_) + (size_t)(i - np1) * 1024, lane * 16, ldsw);
    }
  }
  bulk(a, lds);
}

extern "C" void kernel_launch(void* const* d_in, const int* in_sizes, int n_in, void* d_out, int out_size, void* d_ws, size_t ws_size, hipStream_t stream) {
  Args a;
  a.x = (const u16*)d_in[0]; a.mem0 = (const u16*)d_in[1];
  a.Wq = (const u16*)d_in[2]; a.Wk = (const u16*)d_in[3]; a.Wv = (const u16*)d_in[4]; a.Wo = (const u16*)d_in[5];
  a.Wg = (const u16*)d_in[8]; a.Wu = (const u16*)d_in[9]; a.Wd = (const u16*)d_in[10];
  a.c = (const u16*)d_in[11]; a.wm = (const u16*)d_in[12];
  a.out = (u16*)d_out; a.ws = (unsigned char*)d_ws;
  a.x1 = 750; a.x2 = 2300; a.k0 = 0; a.k1 = 0;
  (void)hipFuncSetAttribute((const void*)k_main, hipFuncAttributeMaxDynamicSharedMemorySize, 160 * 1024);
  k_pre<<<55 + 73, 256, 0, stream>>>(a);
  k_main<<<256, 512, 160 * 1024, stream>>>(a);
}
```
